```python
import torch

def _softmax(x, axis=-1):
    return torch.softmax(x, dim=axis)
def _gelu(x):
    return torch.nn.functional.gelu(x, approximate="tanh")
def _silu(x):
    return torch.nn.functional.silu(x)
def _sigmoid(x):
    return torch.sigmoid(x)
def _layernorm(x, axis=-1, eps=1e-5):
    m = x.mean(dim=axis, keepdim=True)
    v = x.var(dim=axis, keepdim=True, unbiased=False)
    return (x - m) / torch.sqrt(v + eps)
def _scatter_add(tgt, idx, src, axis=0):
    return tgt.clone().index_add_(axis, idx, src)
def _segment_sum(src, seg, n):
    out = torch.zeros((n,) + src.shape[1:], dtype=src.dtype)
    return out.index_add_(0, seg, src)
def _pad(x, pw):
    flat = tuple(v for pair in reversed(pw) for v in pair)
    return torch.nn.functional.pad(x, flat)

kernel_name = 'sequential_preLN__sliding_block__glu_gated_glu__prompt'

def setup_inputs():

    _w = (lambda a: (lambda t: t.to(torch.bfloat16) if t.is_floating_point() else t)(torch.as_tensor(a)))
    def _A_setup():
        B, S, D = 1, 2048, 512
        return {
            "x": _w((lambda *s: torch.randn(*s, dtype=torch.bfloat16) * 0.05)(B, S, D)),
            "wqkv": _w((lambda *s: torch.randn(*s, dtype=torch.bfloat16) * 0.05)(D, 3 * D)),
            "wo": _w((lambda *s: torch.randn(*s, dtype=torch.bfloat16) * 0.05)(D, D)),
        }
    _A_in = _A_setup()

    def _B_setup():
        B, S, D, F = 1, 2048, 512, 1376
        return {
            "x": _w((lambda *s: torch.randn(*s, dtype=torch.bfloat16) * 0.05)(B, S, D)),
            "w_gate": _w((lambda *s: torch.randn(*s, dtype=torch.bfloat16) * 0.05)(D, F)),
            "w_up": _w((lambda *s: torch.randn(*s, dtype=torch.bfloat16) * 0.05)(D, F)),
            "w_down": _w((lambda *s: torch.randn(*s, dtype=torch.bfloat16) * 0.05)(F, D)),
        }
    _B_in = _B_setup()

    return {
        "A_x": _A_in["x"],
        "A_wqkv": _A_in["wqkv"],
        "A_wo": _A_in["wo"],
        "B_x": _B_in["x"],
        "B_w_gate": _B_in["w_gate"],
        "B_w_up": _B_in["w_up"],
        "B_w_down": _B_in["w_down"],
    }

def reference(A_x, A_wqkv, A_wo, B_x, B_w_gate, B_w_up, B_w_down):
    def _A_ref(x, wqkv, wo):
        B, S, D = x.shape
        H = 32; Dh = D // H; W = 256
        qkv = (lambda x, *d: x.permute(*d))((x @ wqkv).reshape(B, S, 3, H, Dh), 2, 0, 3, 1, 4)
        q, k, v = qkv[0], qkv[1], qkv[2]
        scores = torch.einsum("bhqd,bhkd->bhqk", q, k) * (Dh ** -0.5)
        idx = torch.arange(S)
        mask = (idx[:, None] - idx[None, :] >= 0) & (idx[:, None] - idx[None, :] < W)
        scores = torch.where(mask[None, None], scores, -1e9)
        p = _softmax(scores, axis=-1)
        o = torch.einsum("bhqk,bhkd->bhqd", p, v)
        return (lambda x, *d: x.permute(*d))(o, 0, 2, 1, 3).reshape(B, S, D) @ wo

    def _B_ref(x, w_gate, w_up, w_down):
        g = torch.einsum("bsd,df->bsf", x, w_gate)
        u = torch.einsum("bsd,df->bsf", x, w_up)
        h = _sigmoid(g) * u
        return torch.einsum("bsf,fd->bsd", h, w_down)

    x = A_x
    h = x + _A_ref(_layernorm(x, axis=-1), A_wqkv, A_wo)
    h2 = h + _B_ref(_layernorm(h, axis=-1), B_w_gate, B_w_up, B_w_down)
    return h2
```

```cpp
#include <hip/hip_runtime.h>
#include <hip/hip_bf16.h>
typedef __hip_bfloat16 bf16;
constexpr int S=2048, D=512, F=1376, H=32, DH=16, W=256;

__global__ void ln_k(const bf16* x, bf16* y){
  int row=blockIdx.x; int t=threadIdx.x;
  float v[8]; float s=0;
  for(int i=0;i<8;i++){ v[i]=__bfloat162float(x[row*D+t*8+i]); s+=v[i]; }
  for(int m=32;m>=1;m>>=1) s+=__shfl_xor(s,m);
  float mean=s/D; float q=0;
  for(int i=0;i<8;i++){ float d=v[i]-mean; q+=d*d; }
  for(int m=32;m>=1;m>>=1) q+=__shfl_xor(q,m);
  float r=rsqrtf(q/D+1e-5f);
  for(int i=0;i<8;i++) y[row*D+t*8+i]=__float2bfloat16((v[i]-mean)*r);
}
__global__ void gemm_k(const bf16* A, const bf16* B, const bf16* res, bf16* C, int M,int N,int K){
  int n=blockIdx.x*64+threadIdx.x; int m=blockIdx.y;
  if(n>=N) return;
  float acc=0;
  for(int k=0;k<K;k++) acc+=__bfloat162float(A[m*K+k])*__bfloat162float(B[k*N+n]);
  if(res) acc=__bfloat162float(__float2bfloat16(acc))+__bfloat162float(res[m*N+n]);
  C[m*N+n]=__float2bfloat16(acc);
}
__global__ void glu_k(const bf16* A, const bf16* Wg, const bf16* Wu, bf16* C){
  int n=blockIdx.x*64+threadIdx.x; int m=blockIdx.y;
  if(n>=F) return;
  float g=0,u=0;
  for(int k=0;k<D;k++){ float a=__bfloat162float(A[m*D+k]); g+=a*__bfloat162float(Wg[k*F+n]); u+=a*__bfloat162float(Wu[k*F+n]); }
  float sg=1.f/(1.f+__expf(-g));
  C[m*F+n]=__float2bfloat16(sg*u);
}
__global__ void attn_k(const bf16* qkv, bf16* o){
  int q=blockIdx.x; int h=threadIdx.x;
  float qv[16];
  for(int d=0;d<16;d++) qv[d]=__bfloat162float(qkv[q*1536+h*16+d]);
  int k0=q-W+1; if(k0<0)k0=0;
  float m=-1e30f;
  for(int k=k0;k<=q;k++){ float s=0; for(int d=0;d<16;d++) s+=qv[d]*__bfloat162float(qkv[k*1536+512+h*16+d]); s*=0.25f; m=fmaxf(m,s);}
  float l=0; float acc[16]; for(int d=0;d<16;d++) acc[d]=0;
  for(int k=k0;k<=q;k++){ float s=0; for(int d=0;d<16;d++) s+=qv[d]*__bfloat162float(qkv[k*1536+512+h*16+d]); s*=0.25f; float p=__expf(s-m); l+=p; for(int d=0;d<16;d++) acc[d]+=p*__bfloat162float(qkv[k*1536+1024+h*16+d]); }
  for(int d=0;d<16;d++) o[q*D+h*16+d]=__float2bfloat16(acc[d]/l);
}
extern "C" void kernel_launch(void* const* d_in, const int* in_sizes, int n_in, void* d_out, int out_size, void* d_ws, size_t ws_size, hipStream_t stream){
  const bf16* x=(const bf16*)d_in[0]; const bf16* wqkv=(const bf16*)d_in[1]; const bf16* wo=(const bf16*)d_in[2];
  const bf16* wg=(const bf16*)d_in[4]; const bf16* wu=(const bf16*)d_in[5]; const bf16* wd=(const bf16*)d_in[6];
  bf16* ws=(bf16*)d_ws;
  bf16* xn=ws; bf16* qkv=xn+S*D; bf16* o=qkv+S*1536; bf16* h=o+S*D; bf16* hn=h+S*D; bf16* act=hn+S*D;
  ln_k<<<S,64,0,stream>>>(x,xn);
  gemm_k<<<dim3(1536/64,S),64,0,stream>>>(xn,wqkv,nullptr,qkv,S,1536,D);
  attn_k<<<S,32,0,stream>>>(qkv,o);
  gemm_k<<<dim3(D/64,S),64,0,stream>>>(o,wo,x,h,S,D,D);
  ln_k<<<S,64,0,stream>>>(h,hn);
  glu_k<<<dim3((F+63)/64,S),64,0,stream>>>(hn,wg,wu,act);
  gemm_k<<<dim3(D/64,S),64,0,stream>>>(act,wd,h,(bf16*)d_out,S,D,F);
}
```

```cpp
#include <hip/hip_runtime.h>
#include <hip/hip_bf16.h>

typedef unsigned v4u __attribute__((ext_vector_type(4)));
typedef unsigned v2u __attribute__((ext_vector_type(2)));
typedef float f32x2 __attribute__((ext_vector_type(2)));
typedef float f32x4 __attribute__((ext_vector_type(4)));
typedef float f32x16 __attribute__((ext_vector_type(16)));
typedef short bf16x8 __attribute__((ext_vector_type(8)));
typedef short s16x4 __attribute__((ext_vector_type(4)));
typedef __bf16 bf16x2_t __attribute__((ext_vector_type(2)));
#define LAS __attribute__((address_space(3)))
#define DI __device__ __forceinline__

constexpr int S_ = 2048, D_ = 512, F_ = 1376;
constexpr unsigned WS_KV = 0;
constexpr unsigned WS_Q = WS_KV + (4u << 20);
constexpr unsigned WS_O = WS_Q + (2u << 20);
constexpr unsigned WS_H = WS_O + (2u << 20);
constexpr unsigned WS_ACT = WS_H + (2u << 20);
constexpr unsigned WS_END = WS_ACT + 128u * 43u * 1024u;
constexpr int LDS_BYTES = 160 * 1024;

DI v4u mk_rsrc(const void* p, unsigned bytes) {
  unsigned long long a = (unsigned long long)p;
  v4u r; r.x = (unsigned)a; r.y = (unsigned)(a >> 32) & 0xffffu; r.z = bytes; r.w = 0x00020000u; return r;
}
DI unsigned pk(float a, float b) { f32x2 v = {a, b}; bf16x2_t r = __builtin_convertvector(v, bf16x2_t); return __builtin_bit_cast(unsigned, r); }
DI float bfr(float a) { return __uint_as_float(pk(a, a) << 16); }
DI float lo16(unsigned u) { return __uint_as_float(u << 16); }
DI float hi16(unsigned u) { return __uint_as_float(u & 0xffff0000u); }

#define DMA(rsrc, lds_addr, voff, soff, OFF) \
  asm volatile("s_mov_b32 m0, %0\n\ts_nop 0\n\tbuffer_load_dwordx4 %1, %2, %3 offen offset:" #OFF " lds" ::"s"(lds_addr), "v"(voff), "s"(rsrc), "s"(soff) : "memory")
#define GLOAD(dst, rsrc, voff, soff) \
  asm volatile("buffer_load_dwordx4 %0, %1, %2, %3 offen" : "=v"(dst) : "v"(voff), "s"(rsrc), "s"(soff) : "memory")
#define GSTORE(val, rsrc, voff, soff) \
  asm volatile("buffer_store_dwordx4 %0, %1, %2, %3 offen\n\ts_nop 1" ::"v"(val), "v"(voff), "s"(rsrc), "s"(soff) : "memory")
#define WAIT_VM(N) asm volatile("s_waitcnt vmcnt(" #N ")" ::: "memory")
#define BAR_VM(N) asm volatile("s_waitcnt vmcnt(" #N ") lgkmcnt(0)\n\ts_barrier" ::: "memory")
#define BAR_LDS() asm volatile("s_waitcnt lgkmcnt(0)\n\ts_barrier" ::: "memory")
#define PIN(x) asm volatile("" : "+v"(x))
#define MFMA16(a, b, c) __builtin_amdgcn_mfma_f32_16x16x32_bf16((a), (b), (c), 0, 0, 0)
#define MFMA32(a, b, c) __builtin_amdgcn_mfma_f32_32x32x16_bf16((a), (b), (c), 0, 0, 0)

DI s16x4 trd(unsigned a) { return __builtin_amdgcn_ds_read_tr16_b64_v4i16((LAS s16x4*)(size_t)a); }
DI bf16x8 trfrag(unsigned a0, unsigned a1) { s16x4 lo = trd(a0), hi = trd(a1); return __builtin_shufflevector(lo, hi, 0, 1, 2, 3, 4, 5, 6, 7); }
DI v4u lds_r128(unsigned a) { return *(LAS v4u*)(size_t)a; }
DI void lds_w128(unsigned a, v4u v) { *(LAS v4u*)(size_t)a = v; }
DI void lds_w64(unsigned a, v2u v) { *(LAS v2u*)(size_t)a = v; }
DI v2u lds_r64(unsigned a) { return *(LAS v2u*)(size_t)a; }
DI bf16x8 asfrag(v4u v) { return __builtin_bit_cast(bf16x8, v); }

template <int CTRL> DI float dpp_add(float v) {
  int t = __builtin_amdgcn_update_dpp(0, __float_as_int(v), CTRL, 0xf, 0xf, false);
  return v + __int_as_float(t);
}
DI float sum16(float v) { v = dpp_add<0x128>(v); v = dpp_add<0x124>(v); v = dpp_add<0x122>(v); v = dpp_add<0x121>(v); return v; }
DI float swap32_add(float a, float b) { auto r = __builtin_amdgcn_permlane32_swap(__float_as_uint(a), __float_as_uint(b), false, false); return __uint_as_float(r[0]) + __uint_as_float(r[1]); }
DI float swap16_add(float a, float b) { auto r = __builtin_amdgcn_permlane16_swap(__float_as_uint(a), __float_as_uint(b), false, false); return __uint_as_float(r[0]) + __uint_as_float(r[1]); }
DI void reduce8(const float* v, float& u0, float& u1) {
  float t0 = swap32_add(v[0], v[1]), t1 = swap32_add(v[2], v[3]), t2 = swap32_add(v[4], v[5]), t3 = swap32_add(v[6], v[7]);
  u0 = sum16(swap16_add(t0, t1)); u1 = sum16(swap16_add(t2, t3));
}
DI float rdl(float v, int lane) { return __int_as_float(__builtin_amdgcn_readlane(__float_as_int(v), lane)); }

struct Tile { int i, cg; };
DI Tile tile_of(int bid) { int xcd = bid & 7, r = bid >> 3; Tile t; t.i = (xcd >> 1) * 8 + (r >> 2); t.cg = (xcd & 1) * 4 + (r & 3); return t; }

DI void ln_scalars(float mean, float var, float& rinv, float& off) {
  float mb = bfr(mean), vb = bfr(var), t = bfr(vb + 1e-5f), s = bfr(__builtin_amdgcn_sqrtf(t));
  rinv = __builtin_amdgcn_rcpf(s); off = (mean - mb) * rinv;
}

__global__ __launch_bounds__(512, 2) void k_qkv(const void* x, const void* wqkv, void* ws) {
  extern __shared__ __attribute__((aligned(16))) char smem[];
  const unsigned L0 = (unsigned)(size_t)(LAS char*)smem;
  const Tile T = tile_of(blockIdx.x);
  const int tid = threadIdx.x, l = tid & 63, w = __builtin_amdgcn_readfirstlane(tid >> 6);
  const v4u rx = mk_rsrc(x, S_ * D_ * 2), rw = mk_rsrc(wqkv, D_ * 1536 * 2), rws = mk_rsrc(ws, WS_END);
  v4u xr[8];
  {
    unsigned voff = l * 16;
#pragma unroll
    for (int r = 0; r < 8; r++) { unsigned so = (T.i * 64 + w * 8 + r) * 1024; GLOAD(xr[r], rx, voff, so); }
  }
  const int kb = w & 3, ksl = w >> 2;
  unsigned wvoff;
  { int row = l >> 3, c = l & 7; int swz = ((row >> 1) & 1) | ((kb & 1) << 1); wvoff = row * 3072 + ((c ^ (swz << 1)) * 16); }
  auto slot = [&](int ks) -> unsigned { return ks < 8 ? 65536u + ks * 12288u : (ks < 12 ? (ks - 8) * 12288u : 65536u + (ks - 12) * 12288u); };
  auto issue = [&](int c) {
    unsigned s0 = slot(2 * c), s1 = slot(2 * c + 1);
    unsigned dst = L0 + (ksl ? s1 : s0) + kb * 3072;
    unsigned so = (unsigned)((32 * (2 * c + ksl) + 8 * kb) * 3072 + T.cg * 128);
    DMA(rw, dst, wvoff, so, 0); DMA(rw, dst, wvoff, so, 1024); DMA(rw, dst, wvoff, so, 2048);
  };
  issue(0); issue(1); issue(2); issue(3);
  asm volatile("s_waitcnt vmcnt(12)" : "+v"(xr[0]), "+v"(xr[1]), "+v"(xr[2]), "+v"(xr[3]), "+v"(xr[4]), "+v"(xr[5]), "+v"(xr[6]), "+v"(xr[7])::"memory");
  {
    float f[8][8], s[8];
#pragma unroll
    for (int r = 0; r < 8; r++) {
#pragma unroll
      for (int j = 0; j < 4; j++) { f[r][2 * j] = lo16(xr[r][j]); f[r][2 * j + 1] = hi16(xr[r][j]); }
      s[r] = ((f[r][0] + f[r][1]) + (f[r][2] + f[r][3])) + ((f[r][4] + f[r][5]) + (f[r][6] + f[r][7]));
    }
    float u0, u1; reduce8(s, u0, u1);
    u0 *= (1.f / 512.f); u1 *= (1.f / 512.f);
#pragma unroll
    for (int r = 0; r < 8; r++) {
      float m = rdl((r >> 2) ? u1 : u0, 16 * (((r & 1) << 1) | ((r >> 1) & 1)));
      float q = 0.f;
#pragma unroll
      for (int j = 0; j < 8; j++) { f[r][j] -= m; q = __builtin_fmaf(f[r][j], f[r][j], q); }
      s[r] = q;
    }
    float q0, q1; reduce8(s, q0, q1);
    float ri0, of0, ri1, of1;
    ln_scalars(u0, q0 * (1.f / 512.f), ri0, of0); ln_scalars(u1, q1 * (1.f / 512.f), ri1, of1);
#pragma unroll
    for (int r = 0; r < 8; r++) {
      const int ln = 16 * (((r & 1) << 1) | ((r >> 1) & 1));
      float ri = rdl((r >> 2) ? ri1 : ri0, ln), of = rdl((r >> 2) ? of1 : of0, ln);
      v4u o;
#pragma unroll
      for (int j = 0; j < 4; j++) o[j] = pk(__builtin_fmaf(f[r][2 * j], ri, of), __builtin_fmaf(f[r][2 * j + 1], ri, of));
      int R = w * 8 + r, rt = R >> 4, rr = R & 15;
      lds_w128(L0 + ((rt * 16 + (l >> 2)) * 64 + (l & 3) * 16 + (rr ^ (l & 15))) * 16, o);
    }
  }
  BAR_LDS();
  const int rg = w >> 2, hd = w & 3, c16 = l & 15, g = l >> 4;
  bf16x8 xa[2][16];
#pragma unroll
  for (int rt = 0; rt < 2; rt++)
#pragma unroll
    for (int ks = 0; ks < 16; ks++)
      xa[rt][ks] = asfrag(lds_r128(L0 + (((rg * 2 + rt) * 16 + ks) * 64 + g * 16 + (c16 ^ ((ks & 3) * 4 + g))) * 16));
  BAR_LDS();
  issue(4); issue(5);
  f32x4 acc[3][2];
#pragma unroll
  for (int s = 0; s < 3; s++) { acc[s][0] = (f32x4){0, 0, 0, 0}; acc[s][1] = (f32x4){0, 0, 0, 0}; }
  unsigned tb;
  { int q = (l >> 2) & 3, p = l & 3; int swz = ((q >> 1) & 1) | ((g & 1) << 1); tb = L0 + g * 3072 + q * 128 + ((hd ^ swz) * 32) + p * 8; }
  auto compute = [&](int c) {
#pragma unroll
    for (int k2 = 0; k2 < 2; k2++) {
      const int ks = 2 * c + k2; const unsigned sb = tb + slot(ks);
#pragma unroll
      for (int s = 0; s < 3; s++) {
        bf16x8 wf = trfrag(sb + s * 1024, sb + s * 1024 + 512);
#pragma unroll
        for (int rt = 0; rt < 2; rt++) acc[s][rt] = (s < 2) ? MFMA16(wf, xa[rt][ks], acc[s][rt]) : MFMA16(xa[rt][ks], wf, acc[s][rt]);
      }
    }
  };
  BAR_VM(15); compute(0);
  BAR_VM(12); issue(6); compute(1);
  BAR_VM(12); issue(7); compute(2);
  BAR_VM(12); compute(3);
  BAR_VM(9); compute(4);
  BAR_VM(6); compute(5);
  BAR_VM(3); compute(6);
  BAR_VM(0); compute(7);
  const unsigned st = L0 + w * 3072;
  const float QS = 0.25f * 1.4426950408889634f;
#pragma unroll
  for (int rt = 0; rt < 2; rt++) {
    unsigned qk = (g >> 1) * 512 + (16 * rt + c16) * 16 + (g & 1) * 8;
    lds_w64(st + qk, (v2u){pk(acc[0][rt][0] * QS, acc[0][rt][1] * QS), pk(acc[0][rt][2] * QS, acc[0][rt][3] * QS)});
    lds_w64(st + 1024 + qk, (v2u){pk(acc[1][rt][0], acc[1][rt][1]), pk(acc[1][rt][2], acc[1][rt][3])});
    unsigned va = ((rt * 2 + (g & 1)) * 16 + c16) * 16 + (g >> 1) * 8;
    lds_w64(st + 2048 + va, (v2u){pk(acc[2][rt][0], acc[2][rt][1]), pk(acc[2][rt][2], acc[2][rt][3])});
  }
  asm volatile("s_waitcnt lgkmcnt(0)" ::: "memory");
  {
    v4u q = lds_r128(st + l * 16), k = lds_r128(st + 1024 + l * 16), v = lds_r128(st + 2048 + l * 16);
    unsigned th = (unsigned)((T.i * 2 + rg) * 32 + T.cg * 4 + hd);
    unsigned voff = l * 16;
    unsigned sq = WS_Q + th * 1024, sk = WS_KV + th * 2048, sv = sk + 1024;
    GSTORE(q, rws, voff, sq); GSTORE(k, rws, voff, sk); GSTORE(v, rws, voff, sv);
  }
}

DI float mx3(float a, float b, float c) { return __builtin_fmaxf(__builtin_fmaxf(a, b), c); }
DI float tmax(const f32x16& s, float m) {
#pragma unroll
  for (int r = 0; r < 16; r += 2) m = mx3(m, s[r], s[r + 1]);
  return m;
}
__global__ __launch_bounds__(512, 2) void k_attn(void* ws) {
  extern __shared__ __attribute__((aligned(16))) char smem[];
  const unsigned L0 = (unsigned)(size_t)(LAS char*)smem;
  const Tile T = tile_of(blockIdx.x);
  const int tid = threadIdx.x, l = tid & 63, w = __builtin_amdgcn_readfirstlane(tid >> 6);
  const v4u rws = mk_rsrc(ws, WS_END);
  const int qt = w >> 2, hd = w & 3, r32 = l & 31, h = l >> 5;
  const unsigned CONST = 81920;
  const unsigned voff = l * 16;
  v4u qv;
  { unsigned th = (unsigned)((T.i * 2 + qt) * 32 + T.cg * 4 + hd); unsigned so = WS_Q + th * 1024; GLOAD(qv, rws, voff, so); }
#pragma unroll
  for (int tl = 9; tl >= 0; tl--) {
    int Tg = 2 * T.i - 8 + tl;
    if (Tg >= 0) { unsigned so = WS_KV + (unsigned)(Tg * 32 + T.cg * 4) * 2048 + w * 1024; unsigned dst = L0 + tl * 8192 + w * 1024; DMA(rws, dst, voff, so, 0); }
  }
  if (tid < 4) { unsigned v = (tid & 1) ? 0u : 0x3f803f80u; lds_w128(L0 + CONST + (tid & 1) * 16 + (tid >> 1) * 512, (v4u){v, v, v, v}); }
  asm volatile("s_waitcnt vmcnt(0) lgkmcnt(0)\n\ts_barrier" : "+v"(qv)::"memory");
  const bf16x8 qf = asfrag(qv);
  const int nskip = max(0, 8 - (2 * T.i + qt));
  const unsigned kbase = L0 + qt * 8192 + hd * 2048 + l * 16;
  f32x16 zero;
#pragma unroll
  for (int r = 0; r < 16; r++) zero[r] = 0.f;
  bf16x8 kf[9];
  float m = -3.0e38f;
  f32x16 sm;
  {
    kf[8] = asfrag(lds_r128(kbase + 8 * 8192));
    f32x16 s8 = MFMA32(kf[8], qf, zero), s0;
    if (nskip == 0) { kf[0] = asfrag(lds_r128(kbase)); s0 = MFMA32(kf[0], qf, zero); }
    else {
#pragma unroll
      for (int r = 0; r < 16; r++) s0[r] = -3.0e38f;
    }
    const int qq = r32 - 4 * h;
#pragma unroll
    for (int r = 0; r < 16; r++) { const int kc = (r & 3) + 8 * (r >> 2); sm[r] = (qq >= kc) ? s8[r] : s0[r]; }
    m = tmax(sm, m);
  }
#pragma unroll
  for (int kt = 1; kt < 8; kt++) {
    if (kt >= nskip) { kf[kt] = asfrag(lds_r128(kbase + kt * 8192)); f32x16 s = MFMA32(kf[kt], qf, zero); m = tmax(s, m); }
  }
  { auto rr = __builtin_amdgcn_permlane32_swap(__float_as_uint(m), __float_as_uint(m), false, false); m = __builtin_fmaxf(__uint_as_float(rr[0]), __uint_as_float(rr[1])); }
  f32x16 negm;
#pragma unroll
  for (int r = 0; r < 16; r++) negm[r] = -m;
  f32x16 o = zero;
  const unsigned vstride = (r32 < 16) ? 8192u : 0u;
  unsigned va = (r32 < 16) ? (L0 + qt * 8192 + hd * 2048 + 1024 + (h * 16 + r32) * 16) : (L0 + CONST + ((r32 == 16 || r32 == 20) ? 0u : 16u));
  {
    v4u p0, p1;
#pragma unroll
    for (int j = 0; j < 4; j++) {
      p0[j] = pk(__builtin_amdgcn_exp2f(sm[2 * j] - m), __builtin_amdgcn_exp2f(sm[2 * j + 1] - m));
      p1[j] = pk(__builtin_amdgcn_exp2f(sm[8 + 2 * j] - m), __builtin_amdgcn_exp2f(sm[8 + 2 * j + 1] - m));
    }
    const int qq = r32 - 4 * h;
    v4u a0, a1, b0, b1;
#pragma unroll
    for (int j = 0; j < 4; j++) {
      const int kc0 = ((2 * j) & 3) + 8 * ((2 * j) >> 2), kc1 = kc0 + 16;
      unsigned m0 = (qq > kc0) ? 0xffffffffu : ((qq == kc0) ? 0xffffu : 0u);
      unsigned m1 = (qq > kc1) ? 0xffffffffu : ((qq == kc1) ? 0xffffu : 0u);
      a0[j] = p0[j] & m0; b0[j] = p0[j] ^ a0[j]; a1[j] = p1[j] & m1; b1[j] = p1[j] ^ a1[j];
    }
    unsigned v8 = va + 8 * vstride;
    o = MFMA32(asfrag(lds_r128(v8)), asfrag(a0), o); o = MFMA32(asfrag(lds_r128(v8 + 512)), asfrag(a1), o);
    if (nskip == 0) { o = MFMA32(asfrag(lds_r128(va)), asfrag(b0), o); o = MFMA32(asfrag(lds_r128(va + 512)), asfrag(b1), o); }
  }
#pragma unroll
  for (int kt = 1; kt < 8; kt++) {
    va += vstride;
    if (kt >= nskip) {
      f32x16 s = MFMA32(kf[kt], qf, negm);
      v4u p0, p1;
#pragma unroll
      for (int j = 0; j < 4; j++) {
        p0[j] = pk(__builtin_amdgcn_exp2f(s[2 * j]), __builtin_amdgcn_exp2f(s[2 * j + 1]));
        p1[j] = pk(__builtin_amdgcn_exp2f(s[8 + 2 * j]), __builtin_amdgcn_exp2f(s[8 + 2 * j + 1]));
      }
      o = MFMA32(asfrag(lds_r128(va)), asfrag(p0), o); o = MFMA32(asfrag(lds_r128(va + 512)), asfrag(p1), o);
    }
  }
  {
    float inv = __builtin_amdgcn_rcpf(o[8]);
    v4u ov;
#pragma unroll
    for (int j = 0; j < 4; j++) ov[j] = pk(o[2 * j] * inv, o[2 * j + 1] * inv);
    int head = T.cg * 4 + hd;
    unsigned vo = (unsigned)((r32 >> 4) * 16384 + ((2 * (head & 1) + h) * 16 + (r32 & 15)) * 16);
    unsigned so = WS_O + (unsigned)((T.i * 4 + qt * 2) * 16 + (head >> 1)) * 1024;
    GSTORE(ov, rws, vo, so);
  }
}

__global__ __launch_bounds__(512, 2) void k_wo(const void* x, const void* wo, void* ws) {
  extern __shared__ __attribute__((aligned(16))) char smem[];
  const unsigned L0 = (unsigned)(size_t)(LAS char*)smem;
  const Tile T = tile_of(blockIdx.x);
  const int tid = threadIdx.x, l = tid & 63, w = __builtin_amdgcn_readfirstlane(tid >> 6);
  const v4u rx = mk_rsrc(x, S_ * D_ * 2), rw = mk_rsrc(wo, D_ * D_ * 2), rws = mk_rsrc(ws, WS_END);
  const int rt = w >> 1, kh = w & 1, c16 = l & 15, g = l >> 4;
  unsigned wvoff;
  { int row = l >> 3, c = l & 7; wvoff = row * 1024 + ((c ^ (((row >> 1) & 3) << 1)) * 16); }
#pragma unroll
  for (int b = 0; b < 8; b++) { int kbk = b * 8 + w; unsigned so = (unsigned)(kbk * 8 * 1024 + T.cg * 128); unsigned dst = L0 + kbk * 1024; DMA(rw, dst, wvoff, so, 0); }
  { unsigned xv = (l >> 3) * 1024 + (l & 7) * 16; unsigned so = (unsigned)((T.i * 64 + w * 8) * 1024 + T.cg * 128); unsigned dst = L0 + 65536 + w * 1024; DMA(rx, dst, xv, so, 0); }
  v4u of[8];
  {
    unsigned voff = l * 16;
#pragma unroll
    for (int k = 0; k < 8; k++) { unsigned so = WS_O + (unsigned)((T.i * 4 + rt) * 16 + kh * 8 + k) * 1024; GLOAD(of[k], rws, voff, so); }
  }
  asm volatile("s_waitcnt vmcnt(0)\n\ts_barrier" : "+v"(of[0]), "+v"(of[1]), "+v"(of[2]), "+v"(of[3]), "+v"(of[4]), "+v"(of[5]), "+v"(of[6]), "+v"(of[7])::"memory");
  f32x4 acc[4];
#pragma unroll
  for (int n = 0; n < 4; n++) acc[n] = (f32x4){0, 0, 0, 0};
  {
    const int q = (l >> 2) & 3, p = l & 3, row = 4 * (g & 1) + q, swz = (row >> 1) & 3;
    const unsigned tb = L0 + kh * 32768 + (g >> 1) * 2048 + row * 128 + p * 8;
#pragma unroll
    for (int k = 0; k < 8; k++)
#pragma unroll
      for (int n = 0; n < 4; n++) {
        unsigned a = tb + k * 4096 + ((n ^ swz) * 32);
        acc[n] = MFMA16(trfrag(a, a + 1024), asfrag(of[k]), acc[n]);
      }
  }
  const unsigned EX = 73728;
  {
    f32x4 s0 = kh ? acc[0] : acc[2], s1 = kh ? acc[1] : acc[3];
    lds_w128(L0 + EX + (w * 2 + 0) * 1024 + l * 16, __builtin_bit_cast(v4u, s0));
    lds_w128(L0 + EX + (w * 2 + 1) * 1024 + l * 16, __builtin_bit_cast(v4u, s1));
  }
  BAR_LDS();
  {
    f32x4 r0 = __builtin_bit_cast(f32x4, lds_r128(L0 + EX + ((w ^ 1) * 2 + 0) * 1024 + l * 16));
    f32x4 r1 = __builtin_bit_cast(f32x4, lds_r128(L0 + EX + ((w ^ 1) * 2 + 1) * 1024 + l * 16));
    f32x4 a0 = (kh ? acc[2] : acc[0]) + r0, a1 = (kh ? acc[3] : acc[1]) + r1;
    unsigned xa = L0 + 65536 + (rt * 16 + c16) * 128 + kh * 64 + g * 8;
    v2u x0 = lds_r64(xa), x1 = lds_r64(xa + 32);
    v4u hv;
    hv[0] = pk(lo16(x0[0]) + bfr(a0[0]), hi16(x0[0]) + bfr(a0[1])); hv[1] = pk(lo16(x0[1]) + bfr(a0[2]), hi16(x0[1]) + bfr(a0[3]));
    hv[2] = pk(lo16(x1[0]) + bfr(a1[0]), hi16(x1[0]) + bfr(a1[1])); hv[3] = pk(lo16(x1[1]) + bfr(a1[2]), hi16(x1[1]) + bfr(a1[3]));
    unsigned voff = l * 16, so = WS_H + (unsigned)((T.i * 4 + rt) * 16 + T.cg * 2 + kh) * 1024;
    GSTORE(hv, rws, voff, so);
  }
}

__global__ __launch_bounds__(512, 2) void k_ffn1(const void* wg, const void* wu, void* ws) {
  extern __shared__ __attribute__((aligned(16))) char smem[];
  const unsigned L0 = (unsigned)(size_t)(LAS char*)smem;
  const Tile T = tile_of(blockIdx.x);
  const int tid = threadIdx.x, l = tid & 63, w = __builtin_amdgcn_readfirstlane(tid >> 6);
  const v4u rws = mk_rsrc(ws, WS_END);
  const v4u rw = mk_rsrc((w & 1) ? wu : wg, D_ * F_ * 2);
  const int c16 = l & 15, g = l >> 4;
  const int nks = T.cg < 3 ? 6 : 5, ks0 = T.cg < 3 ? 6 * T.cg : 18 + 5 * (T.cg - 3);
  v4u hf[8];
  {
    const int rt = w >> 1, kh = w & 1; unsigned voff = l * 16;
#pragma unroll
    for (int k = 0; k < 8; k++) { unsigned so = WS_H + (unsigned)((T.i * 4 + rt) * 16 + kh * 8 + k) * 1024; GLOAD(hf[k], rws, voff, so); }
  }
  unsigned wvoff;
  { int row = l >> 3, c = l & 7; wvoff = row * 2752 + ((c ^ (((row >> 1) & 3) << 1)) * 16); }
  auto slot = [&](int ks) -> unsigned { return ks < 12 ? 65536u + ks * 8192u : (ks - 12) * 8192u; };
  auto issue = [&](int cb, int ks) {
    unsigned dst = L0 + slot(ks) + w * 1024;
    unsigned so = (unsigned)((32 * ks + 8 * (w >> 1)) * 2752 + (ks0 * 32 + 64 * cb) * 2);
    DMA(rw, dst, wvoff, so, 0);
  };
#pragma unroll
  for (int ks = 0; ks < 12; ks++) issue(0, ks);
  asm volatile("s_waitcnt vmcnt(12)" : "+v"(hf[0]), "+v"(hf[1]), "+v"(hf[2]), "+v"(hf[3]), "+v"(hf[4]), "+v"(hf[5]), "+v"(hf[6]), "+v"(hf[7])::"memory");
  {
    const unsigned STAT = 60 * 1024;
    (void)STAT;
    float f[8][8], s = 0.f;
#pragma unroll
    for (int k = 0; k < 8; k++)
#pragma unroll
      for (int j = 0; j < 4; j++) { f[k][2 * j] = lo16(hf[k][j]); f[k][2 * j + 1] = hi16(hf[k][j]); s += f[k][2 * j] + f[k][2 * j + 1]; }
    s = swap16_add(s, s); s = swap32_add(s, s);
    float mh = s * (1.f / 256.f), q = 0.f;
#pragma unroll
    for (int k = 0; k < 8; k++)
#pragma unroll
      for (int j = 0; j < 8; j++) { f[k][j] -= mh; q = __builtin_fmaf(f[k][j], f[k][j], q); }
    q = swap16_add(q, q); q = swap32_add(q, q);
    const unsigned myb = L0 + ((w >> 1) * 16 + (w & 1) * 8) * 1024, pb = L0 + ((w >> 1) * 16 + ((w & 1) ^ 1) * 8) * 1024;
    if (g == 0) lds_w64(myb + c16 * 8, (v2u){__float_as_uint(mh), __float_as_uint(q)});
    BAR_LDS();
    v2u pv = lds_r64(pb + c16 * 8);
    float mo = __uint_as_float(pv[0]), qo = __uint_as_float(pv[1]);
    float mean = 0.5f * (mh + mo), dl = mh - mo;
    float var = (q + qo + dl * dl * 128.f) * (1.f / 512.f);
    float ri, of; ln_scalars(mean, var, ri, of);
    of += (mh - mean) * ri;
    BAR_LDS();
#pragma unroll
    for (int k = 0; k < 8; k++) {
      v4u o;
#pragma unroll
      for (int j = 0; j < 4; j++) o[j] = pk(__builtin_fmaf(f[k][2 * j], ri, of), __builtin_fmaf(f[k][2 * j + 1], ri, of));
      lds_w128(myb + k * 1024 + l * 16, o);
    }
  }
  BAR_LDS();
  const int t = w & 3, rg = w >> 2;
  bf16x8 ha[2][16];
#pragma unroll
  for (int rt = 0; rt < 2; rt++)
#pragma unroll
    for (int ks = 0; ks < 16; ks++) ha[rt][ks] = asfrag(lds_r128(L0 + ((rg * 2 + rt) * 16 + ks) * 1024 + l * 16));
  BAR_LDS();
#pragma unroll
  for (int ks = 12; ks < 16; ks++) issue(0, ks);
  const unsigned ACTS = 32768;
  unsigned tb;
  { const int q = (l >> 2) & 3, p = l & 3, row = 4 * (g & 1) + q, swz = (row >> 1) & 3; tb = L0 + (g >> 1) * 2048 + row * 128 + ((t ^ swz) * 32) + p * 8; }
  for (int cb = 0; cb < 3; cb++) {
    f32x4 ag[2], au[2];
    ag[0] = ag[1] = au[0] = au[1] = (f32x4){0, 0, 0, 0};
    const bool more = cb < 2;
#pragma unroll
    for (int c = 0; c < 8; c++) {
      if (more) { BAR_VM(12); }
      else {
        switch (c) { case 0: BAR_VM(12); break; case 1: BAR_VM(12); break; case 2: BAR_VM(10); break; case 3: BAR_VM(8); break;
                     case 4: BAR_VM(6); break; case 5: BAR_VM(4); break; case 6: BAR_VM(2); break; default: BAR_VM(0); break; }
      }
      if (c >= 1) { if (more) { issue(cb + 1, 2 * c - 2); issue(cb + 1, 2 * c - 1); } }
      else if (cb >= 1) { issue(cb, 14); issue(cb, 15); }
#pragma unroll
      for (int k2 = 0; k2 < 2; k2++) {
        const int ks = 2 * c + k2; const unsigned a = tb + slot(ks);
        bf16x8 gf = trfrag(a, a + 4096), uf = trfrag(a + 1024, a + 1024 + 4096);
#pragma unroll
        for (int rt = 0; rt < 2; rt++) { ag[rt] = MFMA16(gf, ha[rt][ks], ag[rt]); au[rt] = MFMA16(uf, ha[rt][ks], au[rt]); }
      }
    }
    const int p = 4 * cb + t;
#pragma unroll
    for (int rt = 0; rt < 2; rt++) {
      float a[4];
#pragma unroll
      for (int j = 0; j < 4; j++) { float e = __builtin_amdgcn_exp2f(ag[rt][j] * -1.4426950408889634f); a[j] = au[rt][j] * __builtin_amdgcn_rcpf(1.f + e); }
      lds_w64(L0 + ACTS + ((rg * 2 + rt) * 6 + (p >> 1)) * 1024 + l * 16 + (p & 1) * 8, (v2u){pk(a[0], a[1]), pk(a[2], a[3])});
    }
  }
  BAR_LDS();
#pragma unroll
  for (int b = 0; b < 3; b++) {
    int blk = b * 8 + w, rt = blk / 6, ksl = blk % 6;
    if (ksl < nks) {
      v4u v = lds_r128(L0 + ACTS + blk * 1024 + l * 16);
      unsigned voff = l * 16, so = WS_ACT + (unsigned)((T.i * 4 + rt) * 43 + ks0 + ksl) * 1024;
      GSTORE(v, rws, voff, so);
    }
  }
}

__global__ __launch_bounds__(512, 2) void k_ffn2(const void* wd, void* ws, void* out) {
  extern __shared__ __attribute__((aligned(16))) char smem[];
  const unsigned L0 = (unsigned)(size_t)(LAS char*)smem;
  const Tile T = tile_of(blockIdx.x);
  const int tid = threadIdx.x, l = tid & 63, w = __builtin_amdgcn_readfirstlane(tid >> 6);
  const v4u rw = mk_rsrc(wd, F_ * D_ * 2), rws = mk_rsrc(ws, WS_END), ro = mk_rsrc(out, S_ * D_ * 2);
  const int c16 = l & 15, g = l >> 4, kq = w & 3, rp = w >> 2;
  unsigned wvoff;
  { int row = l >> 3, c = l & 7; wvoff = row * 1024 + ((c ^ (((row >> 1) & 3) << 1)) * 16); }
  auto issue = [&](int m) {
#pragma unroll
    for (int b = 0; b < 4; b++) {
      int blk = m * 32 + b * 8 + w;
      unsigned dst = L0 + (unsigned)(((m & 3) * 32 + b * 8 + w) * 1024);
      unsigned so = (unsigned)(blk * 8 * 1024 + T.cg * 128);
      DMA(rw, dst, wvoff, so, 0);
    }
  };
  v4u af[2][11];
  const unsigned voff = l * 16;
  auto lda = [&](int j) {
    int ks = 4 * j + kq; if (ks > 42) ks = 42;
#pragma unroll
    for (int rt = 0; rt < 2; rt++) { unsigned so = WS_ACT + (unsigned)((T.i * 4 + rp * 2 + rt) * 43 + ks) * 1024; GLOAD(af[rt][j], rws, voff, so); }
  };
  issue(0); lda(0); lda(1); issue(1); lda(2); lda(3); issue(2); lda(4); lda(5); issue(3); lda(6); lda(7); lda(8); lda(9); lda(10);
  { unsigned so = WS_H + (unsigned)((T.i * 4 + (w >> 1)) * 16 + T.cg * 2 + (w & 1)) * 1024; unsigned dst = L0 + 131072 + w * 1024; DMA(rws, dst, voff, so, 0); }
  f32x4 acc[2][4];
#pragma unroll
  for (int rt = 0; rt < 2; rt++)
#pragma unroll
    for (int n = 0; n < 4; n++) acc[rt][n] = (f32x4){0, 0, 0, 0};
  const int q = (l >> 2) & 3, p = l & 3, row = 4 * (g & 1) + q, swz = (row >> 1) & 3;
  const unsigned tb = L0 + kq * 4096 + (g >> 1) * 1024 + row * 128 + p * 8;
  auto compute = [&](int j) {
    const unsigned a = tb + ((j >> 1) & 3) * 32768 + (j & 1) * 16384;
#pragma unroll
    for (int n = 0; n < 4; n++) {
      unsigned b = a + ((n ^ swz) * 32);
      bf16x8 wf = trfrag(b, b + 2048);
#pragma unroll
      for (int rt = 0; rt < 2; rt++) acc[rt][n] = MFMA16(wf, asfrag(af[rt][j]), acc[rt][n]);
    }
  };
#define WAITA(N, j0, j1) asm volatile("s_waitcnt vmcnt(" #N ") lgkmcnt(0)\n\ts_barrier" : "+v"(af[0][j0]), "+v"(af[1][j0]), "+v"(af[0][j1]), "+v"(af[1][j1])::"memory")
  WAITA(31, 0, 1); compute(0); compute(1);
  WAITA(23, 2, 3); issue(4); compute(2); compute(3);
  WAITA(19, 4, 5); issue(5); compute(4); compute(5);
  WAITA(15, 6, 7); compute(6); compute(7);
  WAITA(4, 8, 9); compute(8); compute(9);
  WAITA(0, 10, 10);
  if (kq < 3) compute(10);
  BAR_LDS();
#pragma unroll
  for (int rt = 0; rt < 2; rt++)
#pragma unroll
    for (int n = 0; n < 4; n++)
      lds_w128(L0 + (kq * 64 + (rp * 2 + rt) * 16 + c16) * 272 + (16 * n + 4 * g) * 4, __builtin_bit_cast(v4u, acc[rt][n]));
  BAR_LDS();
  {
    const int tok = tid >> 3, cc = tid & 7;
    f32x4 s0 = {0, 0, 0, 0}, s1 = {0, 0, 0, 0};
#pragma unroll
    for (int k = 0; k < 4; k++) {
      unsigned a = L0 + (k * 64 + tok) * 272 + cc * 32;
      s0 += __builtin_bit_cast(f32x4, lds_r128(a)); s1 += __builtin_bit_cast(f32x4, lds_r128(a + 16));
    }
    const int c0 = 8 * (cc & 3);
    unsigned hb = L0 + 131072 + ((tok >> 4) * 2 + (cc >> 2)) * 1024 + (tok & 15) * 16 + (c0 >> 4) * 8;
    const int g0 = (c0 & 15) >> 2;
    v2u h0 = lds_r64(hb + g0 * 256), h1 = lds_r64(hb + (g0 + 1) * 256);
    v4u ov;
    ov[0] = pk(lo16(h0[0]) + bfr(s0[0]), hi16(h0[0]) + bfr(s0[1])); ov[1] = pk(lo16(h0[1]) + bfr(s0[2]), hi16(h0[1]) + bfr(s0[3]));
    ov[2] = pk(lo16(h1[0]) + bfr(s1[0]), hi16(h1[0]) + bfr(s1[1])); ov[3] = pk(lo16(h1[1]) + bfr(s1[2]), hi16(h1[1]) + bfr(s1[3]));
    unsigned vo = (unsigned)(tok * 1024 + cc * 16), so = (unsigned)(T.i * 64 * 1024 + T.cg * 128);
    GSTORE(ov, ro, vo, so);
  }
}

extern "C" void kernel_launch(void* const* d_in, const int* in_sizes, int n_in, void* d_out, int out_size, void* d_ws, size_t ws_size,
                              hipStream_t stream) {
  hipFuncSetAttribute((const void*)k_qkv, hipFuncAttributeMaxDynamicSharedMemorySize, LDS_BYTES);
  hipFuncSetAttribute((const void*)k_attn, hipFuncAttributeMaxDynamicSharedMemorySize, LDS_BYTES);
  hipFuncSetAttribute((const void*)k_wo, hipFuncAttributeMaxDynamicSharedMemorySize, LDS_BYTES);
  hipFuncSetAttribute((const void*)k_ffn1, hipFuncAttributeMaxDynamicSharedMemorySize, LDS_BYTES);
  hipFuncSetAttribute((const void*)k_ffn2, hipFuncAttributeMaxDynamicSharedMemorySize, LDS_BYTES);
  k_qkv<<<256, 512, LDS_BYTES, stream>>>(d_in[0], d_in[1], d_ws);
  k_attn<<<256, 512, LDS_BYTES, stream>>>(d_ws);
  k_wo<<<256, 512, LDS_BYTES, stream>>>(d_in[0], d_in[2], d_ws);
  k_ffn1<<<256, 512, LDS_BYTES, stream>>>(d_in[4], d_in[5], d_ws);
  k_ffn2<<<256, 512, LDS_BYTES, stream>>>(d_in[6], d_ws, d_out);
}
```

```cpp
#include <hip/hip_runtime.h>
#include <hip/hip_bf16.h>
#include <type_traits>

typedef unsigned v4u __attribute__((ext_vector_type(4)));
typedef unsigned v2u __attribute__((ext_vector_type(2)));
typedef float f32x2 __attribute__((ext_vector_type(2)));
typedef float f32x4 __attribute__((ext_vector_type(4)));
typedef float f32x16 __attribute__((ext_vector_type(16)));
typedef short bf16x8 __attribute__((ext_vector_type(8)));
typedef short s16x4 __attribute__((ext_vector_type(4)));
typedef __bf16 bf16x2_t __attribute__((ext_vector_type(2)));
#define LAS __attribute__((address_space(3)))
#define DI __device__ __forceinline__
#define MAPB 1

constexpr int S_ = 2048, D_ = 512, F_ = 1376;
constexpr unsigned WS_KV = 0;
constexpr unsigned WS_O = WS_KV + (6u << 20);
constexpr unsigned WS_H = WS_O + (2u << 20);
constexpr unsigned WS_ACT = WS_H + (2u << 20);
constexpr unsigned WS_WT = 16u << 20;
constexpr unsigned WS_CP = 19u << 20;
constexpr unsigned WS_FL = 20u << 20;
constexpr unsigned FL_0 = WS_FL;
constexpr unsigned HN_K = FL_0 + 4096;
constexpr unsigned HN_O = HN_K + 8192;
constexpr unsigned HN_H = HN_O + 8192;
constexpr unsigned HN_A = HN_H + 8192;
constexpr unsigned WS_EP = HN_A + 16384;
constexpr unsigned WS_END = WS_EP + 256;
constexpr int LDS_BYTES = 160 * 1024;
constexpr unsigned U = 8192;
constexpr unsigned OOB = 0x7ffffff0u;

DI v4u mk_rsrc(const void* p, unsigned bytes) {
  unsigned long long a = (unsigned long long)p;
  v4u r; r.x = (unsigned)a; r.y = (unsigned)(a >> 32) & 0xffffu; r.z = bytes; r.w = 0x00020000u; return r;
}
DI unsigned pk(float a, float b) { f32x2 v = {a, b}; bf16x2_t r = __builtin_convertvector(v, bf16x2_t); return __builtin_bit_cast(unsigned, r); }
DI unsigned pks(float a, float b) { unsigned u = pk(a, b); return u > 0xfffffffeu ? 0xfffffffeu : u; }
DI float bfr(float a) { return __uint_as_float(pk(a, a) << 16); }
DI float lo16(unsigned u) { return __uint_as_float(u << 16); }
DI float hi16(unsigned u) { return __uint_as_float(u & 0xffff0000u); }
DI float dot_one(unsigned u, float c) { return __builtin_amdgcn_fdot2_f32_bf16(__builtin_bit_cast(bf16x2_t, u), __builtin_bit_cast(bf16x2_t, 0x3f803f80u), c, false); }
DI unsigned umax3(unsigned a, unsigned b, unsigned c) { unsigned m = a > b ? a : b; return m > c ? m : c; }
DI unsigned pchk(v4u v, unsigned m) { return umax3(umax3(m, v[0], v[1]), v[2], v[3]); }

#define DMA(rsrc, lds_addr, voff, soff, OFF) \
  asm volatile("s_mov_b32 m0, %0\n\ts_nop 0\n\tbuffer_load_dwordx4 %1, %2, %3 offen offset:" #OFF " lds" ::"s"(lds_addr), "v"(voff), "s"(rsrc), "s"(soff) : "memory")
#define DMA_SC1(rsrc, lds_addr, voff, soff, OFF) \
  asm volatile("s_mov_b32 m0, %0\n\ts_nop 0\n\tbuffer_load_dwordx4 %1, %2, %3 offen offset:" #OFF " sc1 lds" ::"s"(lds_addr), "v"(voff), "s"(rsrc), "s"(soff) : "memory")
#define GLOAD_SC1(dst, rsrc, voff, soff) \
  asm volatile("buffer_load_dwordx4 %0, %1, %2, %3 offen sc1" : "=v"(dst) : "v"(voff), "s"(rsrc), "s"(soff) : "memory")
#define GSTORE(val, rsrc, voff, soff) \
  asm volatile("buffer_store_dwordx4 %0, %1, %2, %3 offen\n\ts_nop 1" ::"v"(val), "v"(voff), "s"(rsrc), "s"(soff) : "memory")
#define GSTORE_SC1(val, rsrc, voff, soff) \
  asm volatile("buffer_store_dwordx4 %0, %1, %2, %3 offen sc1\n\ts_nop 1" ::"v"(val), "v"(voff), "s"(rsrc), "s"(soff) : "memory")
#define ST64_SC1(val, rsrc, voff, soff) \
  asm volatile("buffer_store_dwordx2 %0, %1, %2, %3 offen sc1\n\ts_nop 1" ::"v"(val), "v"(voff), "s"(rsrc), "s"(soff) : "memory")
#define ST32_SC1(val, rsrc, voff, soff) \
  asm volatile("buffer_store_dword %0, %1, %2, %3 offen sc1" ::"v"(val), "v"(voff), "s"(rsrc), "s"(soff) : "memory")
#define WAIT_VM(N) asm volatile("s_waitcnt vmcnt(" #N ")" ::: "memory")
#define BAR_VM(N) asm volatile("s_waitcnt vmcnt(" #N ") lgkmcnt(0)\n\ts_barrier" ::: "memory")
#define BAR_LDS() asm volatile("s_waitcnt lgkmcnt(0)\n\ts_barrier" ::: "memory")
#define SB() __builtin_amdgcn_sched_barrier(0)
#define MFMA16(a, b, c) __builtin_amdgcn_mfma_f32_16x16x32_bf16((a), (b), (c), 0, 0, 0)
#define MFMA32(a, b, c) __builtin_amdgcn_mfma_f32_32x32x16_bf16((a), (b), (c), 0, 0, 0)
#define STAT(u, s0, s1, q0, q1) do { float lo_, hi_; \
  asm volatile("v_lshlrev_b32 %0, 16, %6\n\tv_and_b32 %1, 0xffff0000, %6\n\tv_add_f32 %2, %2, %0\n\tv_add_f32 %3, %3, %1\n\tv_fmac_f32 %4, %0, %0\n\tv_fmac_f32 %5, %1, %1" \
               : "=&v"(lo_), "=&v"(hi_), "+v"(s0), "+v"(s1), "+v"(q0), "+v"(q1) : "v"(u)); } while (0)

typedef LAS char* ldsp;
DI s16x4 trd(ldsp a) { return __builtin_amdgcn_ds_read_tr16_b64_v4i16((LAS s16x4*)a); }
DI bf16x8 trfrag(ldsp a0, ldsp a1) { s16x4 lo = trd(a0), hi = trd(a1); return __builtin_shufflevector(lo, hi, 0, 1, 2, 3, 4, 5, 6, 7); }
DI v4u lds_r128(ldsp a) { return *(LAS v4u*)a; }
DI void lds_w128(ldsp a, v4u v) { *(LAS v4u*)a = v; }
DI void lds_w64(ldsp a, v2u v) { *(LAS v2u*)a = v; }
DI v2u lds_r64(ldsp a) { return *(LAS v2u*)a; }
DI bf16x8 asfrag(v4u v) { return __builtin_bit_cast(bf16x8, v); }
DI v4u asu(bf16x8 v) { return __builtin_bit_cast(v4u, v); }
DI f32x4 asf(v4u v) { return __builtin_bit_cast(f32x4, v); }

template <int CTRL> DI float dpp_add(float v) {
  int t = __builtin_amdgcn_update_dpp(0, __float_as_int(v), CTRL, 0xf, 0xf, false);
  return v + __int_as_float(t);
}
DI float sum16(float v) { v = dpp_add<0x128>(v); v = dpp_add<0x124>(v); v = dpp_add<0x122>(v); v = dpp_add<0x121>(v); return v; }
DI f32x4 sum16x4(v4u v) {
  asm volatile("s_nop 1\n\t"
    "v_add_f32_dpp %0, %0, %0 row_ror:8 row_mask:0xf bank_mask:0xf\n\tv_add_f32_dpp %1, %1, %1 row_ror:8 row_mask:0xf bank_mask:0xf\n\t"
    "v_add_f32_dpp %2, %2, %2 row_ror:8 row_mask:0xf bank_mask:0xf\n\tv_add_f32_dpp %3, %3, %3 row_ror:8 row_mask:0xf bank_mask:0xf\n\t"
    "v_add_f32_dpp %0, %0, %0 row_ror:4 row_mask:0xf bank_mask:0xf\n\tv_add_f32_dpp %1, %1, %1 row_ror:4 row_mask:0xf bank_mask:0xf\n\t"
    "v_add_f32_dpp %2, %2, %2 row_ror:4 row_mask:0xf bank_mask:0xf\n\tv_add_f32_dpp %3, %3, %3 row_ror:4 row_mask:0xf bank_mask:0xf\n\t"
    "v_add_f32_dpp %0, %0, %0 row_ror:2 row_mask:0xf bank_mask:0xf\n\tv_add_f32_dpp %1, %1, %1 row_ror:2 row_mask:0xf bank_mask:0xf\n\t"
    "v_add_f32_dpp %2, %2, %2 row_ror:2 row_mask:0xf bank_mask:0xf\n\tv_add_f32_dpp %3, %3, %3 row_ror:2 row_mask:0xf bank_mask:0xf\n\t"
    "v_add_f32_dpp %0, %0, %0 row_ror:1 row_mask:0xf bank_mask:0xf\n\tv_add_f32_dpp %1, %1, %1 row_ror:1 row_mask:0xf bank_mask:0xf\n\t"
    "v_add_f32_dpp %2, %2, %2 row_ror:1 row_mask:0xf bank_mask:0xf\n\tv_add_f32_dpp %3, %3, %3 row_ror:1 row_mask:0xf bank_mask:0xf"
    : "+v"(v[0]), "+v"(v[1]), "+v"(v[2]), "+v"(v[3]));
  return __builtin_bit_cast(f32x4, v);
}
DI float swap32_add(float a, float b) { auto r = __builtin_amdgcn_permlane32_swap(__float_as_uint(a), __float_as_uint(b), false, false); return __uint_as_float(r[0]) + __uint_as_float(r[1]); }
DI float swap16_add(float a, float b) { auto r = __builtin_amdgcn_permlane16_swap(__float_as_uint(a), __float_as_uint(b), false, false); return __uint_as_float(r[0]) + __uint_as_float(r[1]); }
DI float sum_g(float v) { v = swap16_add(v, v); return swap32_add(v, v); }
DI v4u join16(v2u a, v2u b) {
  auto r0 = __builtin_amdgcn_permlane16_swap(a[0], b[0], false, false), r1 = __builtin_amdgcn_permlane16_swap(a[1], b[1], false, false);
  return (v4u){r0[0], r1[0], r0[1], r1[1]};
}
DI v4u join32(v2u a, v2u b) {
  auto r0 = __builtin_amdgcn_permlane32_swap(a[0], b[0], false, false), r1 = __builtin_amdgcn_permlane32_swap(a[1], b[1], false, false);
  return (v4u){r0[0], r1[0], r0[1], r1[1]};
}
DI void ln_scalars(float s, float q, float& rinv, float& mr) {
  float mean = s * (1.f / 512.f);
  float var = __builtin_fmaxf(__builtin_fmaf(-mean, mean, q * (1.f / 512.f)), 0.f);
  float mb = bfr(mean), vb = bfr(var), t = bfr(vb + 1e-5f), sd = bfr(__builtin_amdgcn_sqrtf(t));
  rinv = __builtin_amdgcn_rcpf(sd); mr = mb * rinv;
}
DI float mx3(float a, float b, float c) { return __builtin_fmaxf(__builtin_fmaxf(a, b), c); }
DI float tmax(const f32x16& s, float m) {
#pragma unroll
  for (int r = 0; r < 16; r += 2) m = mx3(m, s[r], s[r + 1]);
  return m;
}
DI void pexp(const f32x16& s, v4u& p0, v4u& p1) {
#pragma unroll
  for (int j = 0; j < 4; j++) {
    p0[j] = pk(__builtin_amdgcn_exp2f(s[2 * j]), __builtin_amdgcn_exp2f(s[2 * j + 1]));
    p1[j] = pk(__builtin_amdgcn_exp2f(s[8 + 2 * j]), __builtin_amdgcn_exp2f(s[8 + 2 * j + 1]));
  }
}

#define STAMP(idx)
constexpr int SPIN = 40000;

DI void wait_hint(v4u rws, unsigned voff, unsigned soff, unsigned tag) {
#pragma unroll 1
  for (int spin = 0; spin < SPIN; ++spin) {
    unsigned f;
    asm volatile("buffer_load_dword %0, %1, %2, %3 offen sc1\n\ts_waitcnt vmcnt(0)" : "=&v"(f) : "v"(voff), "s"(rws), "s"(soff) : "memory");
    if (__all(f == tag)) break;
    __builtin_amdgcn_s_sleep(2);
  }
}
DI void fetch8k(v4u rws, unsigned so, ldsp L, unsigned dst, unsigned L0, unsigned v16) {
#pragma unroll 1
  for (int spin = 0; spin < SPIN; ++spin) {
    unsigned s2 = so, d2 = L0 + dst;
    DMA_SC1(rws, d2, v16, s2, 0); DMA_SC1(rws, d2, v16, s2, 1024); DMA_SC1(rws, d2, v16, s2, 2048); DMA_SC1(rws, d2, v16, s2, 3072);
    s2 += 4096; d2 += 4096;
    DMA_SC1(rws, d2, v16, s2, 0); DMA_SC1(rws, d2, v16, s2, 1024); DMA_SC1(rws, d2, v16, s2, 2048); DMA_SC1(rws, d2, v16, s2, 3072);
    WAIT_VM(0);
    unsigned m = 0;
#pragma unroll
    for (int k = 0; k < 8; k++) m = pchk(lds_r128(L + dst + v16 + k * 1024), m);
    if (!__any(m == 0xffffffffu)) break;
  }
}

constexpr int P1 = 24;

__global__ __launch_bounds__(512, 2) __attribute__((amdgpu_waves_per_eu(2, 2)))
void fused_block(const void* x, const void* wqkv, const void* wo, const void* wg, const void* wu, const void* wd, void* ws, void* out) {
  extern __shared__ __attribute__((aligned(16))) char smem[];
  const ldsp L = (ldsp)smem;
  const unsigned L0 = (unsigned)(size_t)L;
  const int bid = blockIdx.x;
  const int ti = ((bid & 7) >> 1) * 8 + (bid >> 5), cg = (bid & 1) * 4 + ((bid >> 3) & 3);
  const int tj = MAPB ? (bid & 7) * 4 + (bid >> 6) : ti, ch = MAPB ? (bid >> 3) & 7 : cg;
  const int tid = threadIdx.x, l = tid & 63, w = __builtin_amdgcn_readfirstlane(tid >> 6);
  const int c16 = l & 15, g = l >> 4, q4 = (l >> 2) & 3, p4 = l & 3;
  const int gw = bid * 8 + w;
  const v4u rx = mk_rsrc(x, S_ * D_ * 2), rwq = mk_rsrc(wqkv, D_ * 1536 * 2), rwo = mk_rsrc(wo, D_ * D_ * 2);
  const v4u rgu = mk_rsrc(((gw >> 4) & 1) ? wu : wg, D_ * F_ * 2), rwd = mk_rsrc(wd, F_ * D_ * 2);
  const v4u rws = mk_rsrc(ws, WS_END), rout = mk_rsrc(out, S_ * D_ * 2);
  const unsigned v16 = l * 16;
  const int rg = w >> 2, hd = w & 3;
  const int nks = ch < 3 ? 6 : 5, ks0 = ch < 3 ? 6 * ch : 18 + 5 * (ch - 3);
  STAMP(0);
  v2u ep;
  { unsigned z = 0, so = WS_EP; asm volatile("buffer_load_dwordx2 %0, %1, %2, %3 offen sc1" : "=v"(ep) : "v"(z), "s"(rws), "s"(so) : "memory"); }
  const bool do_pt = gw < 1376;
  const unsigned PTS = 17 * U + w * 2048;
  if (do_pt) {
    unsigned vo = (l >> 2) * 2752 + (l & 3) * 16, so = (unsigned)((gw & 15) * 32 * 2752 + (gw >> 5) * 64), dst = L0 + PTS;
    DMA(rgu, dst, vo, so, 0);
    so += 16 * 2752; dst += 1024;
    DMA(rgu, dst, vo, so, 0);
  }
  const int kb = w & 3, ksl = w >> 2;
  unsigned qvoff, xvoff;
  { int row = l >> 3, c = l & 7; int swz = ((row >> 1) & 1) | ((kb & 1) << 1); qvoff = row * 3072 + ((c ^ (swz << 1)) * 16);
    int r16 = (w & 1) * 8 + row; xvoff = row * 1024 + ((c ^ ((r16 >> 1) & 7)) * 16); }
  auto xissue = [&](int c) {
    unsigned dst = L0 + c * U + w * 1024, so = (unsigned)((ti * 64 + w * 8) * 1024 + c * 128);
    DMA(rx, dst, xvoff, so, 0);
  };
  auto qissue = [&](int c) {
    unsigned dst = L0 + 8 * U + ((2 * c) & 7) * 12288 + ksl * 12288 + kb * 3072;
    unsigned so = (unsigned)((32 * (2 * c + ksl) + 8 * kb) * 3072 + cg * 128);
    DMA(rwq, dst, qvoff, so, 0); DMA(rwq, dst, qvoff, so, 1024); DMA(rwq, dst, qvoff, so, 2048);
  };
  xissue(0); qissue(0); xissue(1); qissue(1); xissue(2); qissue(2); xissue(3); xissue(4); xissue(5); xissue(6); xissue(7);
  const unsigned so_kv = WS_KV + (unsigned)((ti * 2 + rg) * 32 + cg * 4 + hd) * 2048;
  const unsigned so_o = WS_O + (unsigned)((ti * 4 + rg * 2) * 16 + cg * 2 + (hd >> 1)) * 1024;
  const unsigned vo_o = (unsigned)(((l & 31) >> 4) * 16384 + ((2 * (hd & 1) + (l >> 5)) * 16 + c16) * 16);
  const unsigned so_h = WS_H + (unsigned)((tj * 4 + (w >> 1)) * 16 + ch * 2 + (w & 1)) * 1024;
  {
    const v4u PZ = {0xffffffffu, 0xffffffffu, 0xffffffffu, 0xffffffffu};
    unsigned s2 = so_kv + 1024;
    GSTORE_SC1(PZ, rws, v16, so_kv); GSTORE_SC1(PZ, rws, v16, s2); GSTORE_SC1(PZ, rws, vo_o, so_o); GSTORE_SC1(PZ, rws, v16, so_h);
#pragma unroll
    for (int b = 0; b < 3; b++) {
      int idx = 3 * w + b, tg = idx >= 12 ? 1 : 0, p = idx - 12 * tg;
      unsigned so = p < 2 * nks ? WS_ACT + (unsigned)((tj * 2 + tg) * 86 + 2 * ks0 + p) * 1024 : OOB;
      GSTORE_SC1(PZ, rws, v16, so);
    }
  }
  asm volatile("s_waitcnt vmcnt(24)" : "+v"(ep)::"memory");
  const unsigned long long epoch = ((unsigned long long)(unsigned)__builtin_amdgcn_readfirstlane(ep[1]) << 32) | (unsigned)__builtin_amdgcn_readfirstlane(ep[0]);
  v2u tag;
  { unsigned long long e = epoch * 0x9E3779B97F4A7C15ull + 0x632BE59BD9B4E019ull; tag[0] = (unsigned)e; tag[1] = (unsigned)(e >> 32); }
  const unsigned htag = tag[1];
  if (do_pt) {
    const ldsp a = L + PTS + (4 * g + q4) * 64 + p4 * 8;
    v4u f0 = asu(trfrag(a, a + 1024)), f1 = asu(trfrag(a + 32, a + 32 + 1024));
    const unsigned t0 = (unsigned)((gw >> 5) * 4 + ((gw >> 4) & 1));
    unsigned so = WS_WT + (t0 * 16 + (gw & 15)) * 1024;
    GSTORE_SC1(f0, rws, v16, so);
    so += 32 * 1024;
    GSTORE_SC1(f1, rws, v16, so);
    float c0 = 0.f, c1 = 0.f;
#pragma unroll
    for (int j = 0; j < 4; j++) { c0 = dot_one(f0[j], c0); c1 = dot_one(f1[j], c1); }
    c0 = sum_g(c0); c1 = sum_g(c1);
    unsigned cv = __float_as_uint(g == 0 ? c0 : c1), vo = g < 2 ? (unsigned)(g * 2048 + c16 * 4) : OOB;
    unsigned sc = WS_CP + (t0 * 16 + (gw & 15)) * 64;
    ST32_SC1(cv, rws, vo, sc);
  }
  STAMP(1);
  bf16x8 qf;
  {
    f32x4 acc[3][2], cs[3];
#pragma unroll
    for (int s = 0; s < 3; s++) { acc[s][0] = (f32x4){0, 0, 0, 0}; acc[s][1] = (f32x4){0, 0, 0, 0}; cs[s] = (f32x4){0, 0, 0, 0}; }
    const bf16x8 ones = asfrag((v4u){0x3f803f80u, 0x3f803f80u, 0x3f803f80u, 0x3f803f80u});
    float s0 = 0.f, s1 = 0.f, q0 = 0.f, q1 = 0.f;
    ldsp tb0, tb1;
    { int swz = ((q4 >> 1) & 1) | ((g & 1) << 1); tb0 = L + 8 * U + g * 3072 + q4 * 128 + ((hd ^ swz) * 32) + p4 * 8; tb1 = tb0 + 49152; }
    const unsigned xo0 = (c16 >> 3) * 1024 + (c16 & 7) * 128 + ((g ^ (c16 >> 1)) * 16), xo1 = xo0 ^ 64;
    const ldsp xb0 = L + rg * 4096 + xo0, xb1 = L + rg * 4096 + xo1;
    const ldsp xbs = L + (w >> 1) * 2048 + ((w & 1) ? xo1 : xo0);
    const ldsp TB = L + 8 * U;
    bf16x8 wf[2][3], xa[2][2];
    v4u xs[2];
    auto rd = [&](int ks) {
      const int sl = ks & 7, b = ks & 1;
      const ldsp t = sl < 4 ? tb0 + sl * 12288 : tb1 + (sl - 4) * 12288;
      const ldsp xp = (b ? xb1 : xb0) + (ks >> 1) * U;
      xa[b][0] = asfrag(lds_r128(xp)); xa[b][1] = asfrag(lds_r128(xp + 2048));
#pragma unroll
      for (int s = 0; s < 3; s++) wf[b][s] = trfrag(t + s * 1024, t + s * 1024 + 512);
    };
#pragma unroll
    for (int ks = 0; ks < 16; ks++) {
      const int b = ks & 1;
      const bool st = ks >= 5 && ks <= 12;
      if (ks == 0) { BAR_VM(16); rd(0); }
      if (ks == 2) BAR_VM(15);
      if (ks == 4 || ks == 6 || ks == 8 || ks == 10) BAR_VM(3);
      if (ks == 12) BAR_VM(0);
      if (ks == 14) BAR_LDS();
      SB();
      acc[0][0] = MFMA16(wf[b][0], xa[b][0], acc[0][0]);
      SB();
      if (ks < 15) rd(ks + 1);
      if (ks >= 4 && ks <= 11) xs[b ^ 1] = lds_r128(xbs + (ks - 4) * U);
      SB();
      acc[0][1] = MFMA16(wf[b][0], xa[b][1], acc[0][1]);
      SB();
      if (b == 0 && ks <= 8) qissue(ks / 2 + 3);
      if (ks == 4 && w == 0) { unsigned z = 0, so = FL_0 + (unsigned)bid * 8; ST64_SC1(tag, rws, z, so); }
      SB();
      acc[1][0] = MFMA16(wf[b][1], xa[b][0], acc[1][0]);
      acc[1][1] = MFMA16(wf[b][1], xa[b][1], acc[1][1]);
      SB(); if (st) STAT(xs[b][0], s0, s1, q0, q1); SB();
      acc[2][0] = MFMA16(xa[b][0], wf[b][2], acc[2][0]);
      acc[2][1] = MFMA16(xa[b][1], wf[b][2], acc[2][1]);
      SB(); if (st) STAT(xs[b][1], s0, s1, q0, q1); SB();
      cs[0] = MFMA16(wf[b][0], ones, cs[0]);
      cs[1] = MFMA16(wf[b][1], ones, cs[1]);
      SB(); if (st) STAT(xs[b][2], s0, s1, q0, q1); SB();
      cs[2] = MFMA16(ones, wf[b][2], cs[2]);
      SB(); if (st) STAT(xs[b][3], s0, s1, q0, q1); SB();
      if (ks == 13) {
        float ss = sum_g(s0 + s1), qq = sum_g(q0 + q1);
        if (g == 0) lds_w64(TB + (w & 1) * 512 + ((w >> 1) * 16 + c16) * 8, (v2u){__float_as_uint(ss), __float_as_uint(qq)});
      }
      if (ks == 14) {
        const int tok = rg * 32 + (g & 1) * 16 + c16;
        v2u a = lds_r64(TB + tok * 8), b2 = lds_r64(TB + 512 + tok * 8);
        float rinv, mr;
        ln_scalars(__uint_as_float(a[0]) + __uint_as_float(b2[0]), __uint_as_float(a[1]) + __uint_as_float(b2[1]), rinv, mr);
        if (g < 2) lds_w64(TB + 4096 + w * 256 + (g * 16 + c16) * 8, (v2u){__float_as_uint(rinv), __float_as_uint(mr)});
      }
    }
    STAMP(2);
    const float QS = 0.25f * 1.4426950408889634f;
    v2u qp[2], kp[2], vp[2];
#pragma unroll
    for (int rt = 0; rt < 2; rt++) {
      v2u sc = lds_r64(TB + 4096 + w * 256 + (rt * 16 + c16) * 8);
      f32x4 s01 = asf(lds_r128(TB + 4096 + w * 256 + (rt * 16 + 4 * g) * 8)), s23 = asf(lds_r128(TB + 4096 + w * 256 + (rt * 16 + 4 * g) * 8 + 16));
      const float ri = __uint_as_float(sc[0]), mr = __uint_as_float(sc[1]);
      f32x4 kv = acc[1][rt] * ri - cs[1] * mr, qv = (acc[0][rt] * ri - cs[0] * mr) * QS;
      kp[rt] = (v2u){pks(kv[0], kv[1]), pks(kv[2], kv[3])};
      qp[rt] = (v2u){pk(qv[0], qv[1]), pk(qv[2], qv[3])};
      const float cv = cs[2][0];
      float v0 = acc[2][rt][0] * s01[0] - cv * s01[1], v1 = acc[2][rt][1] * s01[2] - cv * s01[3];
      float v2 = acc[2][rt][2] * s23[0] - cv * s23[1], v3 = acc[2][rt][3] * s23[2] - cv * s23[3];
      vp[rt] = (v2u){pks(v0, v1), pks(v2, v3)};
    }
    v4u kk = join16(kp[0], kp[1]), vv = join32(vp[0], vp[1]);
    { unsigned s2 = so_kv + 1024; GSTORE_SC1(kk, rws, v16, so_kv); GSTORE_SC1(vv, rws, v16, s2);
      unsigned z = 0, sh = HN_K + (unsigned)((cg * 64 + ti * 2 + rg) * 4 + hd) * 4; ST32_SC1(htag, rws, z, sh); }
    const ldsp kvs = L + (9 + rg) * U + hd * 2048 + v16;
    lds_w128(kvs, kk); lds_w128(kvs + 1024, vv);
    qf = asfrag(join16(qp[0], qp[1]));
  }
  STAMP(3);
  if (w == 0) {
    int Tg = 2 * ti - 8 + ((l & 31) >> 2); Tg = Tg < 0 ? 2 * ti : Tg;
    wait_hint(rws, (unsigned)((Tg * 4 + (l & 3)) * 4), HN_K + (unsigned)cg * 1024, htag);
  }
  if (w == 7) {
#pragma unroll 1
    for (int spin = 0; spin < SPIN; ++spin) {
      v4u f0, f1; unsigned vo = l * 32, so = FL_0;
      asm volatile("buffer_load_dwordx4 %0, %2, %3, %4 offen sc1\n\tbuffer_load_dwordx4 %1, %2, %3, %4 offen offset:16 sc1\n\ts_waitcnt vmcnt(0)"
                   : "=&v"(f0), "=&v"(f1) : "v"(vo), "s"(rws), "s"(so) : "memory");
      bool ok = f0[0] == tag[0] && f0[1] == tag[1] && f0[2] == tag[0] && f0[3] == tag[1] && f1[0] == tag[0] && f1[1] == tag[1] && f1[2] == tag[0] && f1[3] == tag[1];
      if (__all(ok)) break;
      __builtin_amdgcn_s_sleep(4);
    }
    if (bid == 0) { unsigned long long e = epoch + 1ull; v2u en = {(unsigned)e, (unsigned)(e >> 32)}; unsigned z = 0, so = WS_EP; ST64_SC1(en, rws, z, so); }
  }
  const unsigned CONST = 11 * U;
  if (tid >= 64 && tid < 68) { unsigned v = (tid & 1) ? 0u : 0x3f803f80u; lds_w128(L + CONST + (tid & 1) * 16 + ((tid >> 1) & 1) * 512, (v4u){v, v, v, v}); }
  BAR_LDS();
  STAMP(4);
  { const int Tg = 2 * ti - 8 + w; if (Tg >= 0) fetch8k(rws, WS_KV + (unsigned)(Tg * 32 + cg * 4) * 2048, L, (1 + w) * U, L0, v16); }
  unsigned ovoff, dvoff;
  { int row = l >> 3, c = l & 7; ovoff = row * 1024 + ((c ^ (((row >> 1) & 3) << 1)) * 16);
    int swz = ((row >> 1) & 1) | ((w & 1) << 1); dvoff = row * 1024 + ((c ^ (swz << 1)) * 16); }
  BAR_LDS();
  STAMP(5);
#pragma unroll
  for (int b = 0; b < 8; b++) { int kbk = b * 8 + w; unsigned so = (unsigned)(kbk * 8192 + ch * 128); unsigned dst = L0 + 12 * U + kbk * 1024; DMA(rwo, dst, ovoff, so, 0); }
  { unsigned xv = (l >> 3) * 1024 + (l & 7) * 16; unsigned so = (unsigned)((tj * 64 + w * 8) * 1024 + ch * 128); unsigned dst = L0 + w * 1024; DMA(rx, dst, xv, so, 0); }
  v4u wq[32];
  const bool two = 8 + w < 2 * nks;
  const unsigned wt0 = WS_WT + (unsigned)(2 * ks0 + w) * 32768, wt1 = wt0 + 8 * 32768;
  auto wload = [&](int n, unsigned base) { unsigned so = base + (unsigned)((n & 1) * 16384 + (n >> 1) * 1024); GLOAD_SC1(wq[n], rws, v16, so); };
#pragma unroll
  for (int n = 0; n < P1; n++) wload(n, wt0);
  {
    const int qt = rg, r32 = l & 31, h = l >> 5;
    const int nskip = max(0, 8 - (2 * ti + qt));
    const ldsp kbase = L + (1 + qt) * U + hd * 2048 + v16;
    f32x16 zero;
#pragma unroll
    for (int r = 0; r < 16; r++) zero[r] = 0.f;
    const int qq = r32 - 4 * h;
    const unsigned vstride = (r32 < 16) ? U : 0u;
    const ldsp va = (r32 < 16) ? (L + (1 + qt) * U + hd * 2048 + 1024 + (h * 16 + r32) * 16) : (L + CONST + ((r32 == 16 || r32 == 20) ? 0u : 16u));
    f32x16 o = zero, sm;
    float m;
    if (nskip == 0) {
      bf16x8 kf[9];
#pragma unroll
      for (int kt = 0; kt < 8; kt++) kf[kt] = asfrag(lds_r128(kbase + kt * U));
      kf[8] = asfrag(lds_r128(kbase + 4 * U + 4 * U));
      SB();
      {
        f32x16 s8 = MFMA32(kf[8], qf, zero), s0 = MFMA32(kf[0], qf, zero);
        f32x16 sa = MFMA32(kf[1], qf, zero), sb = MFMA32(kf[2], qf, zero);
        SB();
#pragma unroll
        for (int r = 0; r < 16; r++) { const int kc = (r & 3) + 8 * (r >> 2); sm[r] = (qq >= kc) ? s8[r] : s0[r]; }
        s8 = MFMA32(kf[3], qf, zero); s0 = MFMA32(kf[4], qf, zero);
        float m1 = tmax(sm, -3.0e38f), m2 = tmax(sa, -3.0e38f);
        sa = MFMA32(kf[5], qf, zero);
        m1 = tmax(sb, m1);
        sb = MFMA32(kf[6], qf, zero);
        m2 = tmax(s8, m2);
        s8 = MFMA32(kf[7], qf, zero);
        m1 = tmax(s0, m1); m2 = tmax(sa, m2); m1 = tmax(sb, m1); m2 = tmax(s8, m2);
        m = __builtin_fmaxf(m1, m2);
      }
      { auto rr = __builtin_amdgcn_permlane32_swap(__float_as_uint(m), __float_as_uint(m), false, false); m = __builtin_fmaxf(__uint_as_float(rr[0]), __uint_as_float(rr[1])); }
      f32x16 negm;
#pragma unroll
      for (int r = 0; r < 16; r++) negm[r] = -m;
      f32x16 sc = MFMA32(kf[1], qf, negm), sn;
      v4u vf[2][2];
      {
        ldsp v8 = va + 8 * vstride;
        v4u v80 = lds_r128(v8), v81 = lds_r128(v8 + 512), v00 = lds_r128(va), v01 = lds_r128(va + 512);
        vf[1][0] = lds_r128(va + vstride); vf[1][1] = lds_r128(va + vstride + 512);
#pragma unroll
        for (int r = 0; r < 16; r++) sm[r] -= m;
        v4u p0, p1; pexp(sm, p0, p1);
        v4u a0, a1, b0, b1;
#pragma unroll
        for (int j = 0; j < 4; j++) {
          const int kc0 = ((2 * j) & 3) + 8 * ((2 * j) >> 2), kc1 = kc0 + 16;
          unsigned m0 = (qq > kc0) ? 0xffffffffu : ((qq == kc0) ? 0xffffu : 0u);
          unsigned m1 = (qq > kc1) ? 0xffffffffu : ((qq == kc1) ? 0xffffu : 0u);
          a0[j] = p0[j] & m0; b0[j] = p0[j] ^ a0[j]; a1[j] = p1[j] & m1; b1[j] = p1[j] ^ a1[j];
        }
        o = MFMA32(asfrag(v80), asfrag(a0), o); o = MFMA32(asfrag(v81), asfrag(a1), o);
        o = MFMA32(asfrag(v00), asfrag(b0), o); o = MFMA32(asfrag(v01), asfrag(b1), o);
      }
#pragma unroll
      for (int kt = 1; kt < 8; kt++) {
        if (kt < 7) {
          sn = MFMA32(kf[kt + 1], qf, negm);
          ldsp vn = va + (kt + 1) * vstride;
          vf[(kt + 1) & 1][0] = lds_r128(vn); vf[(kt + 1) & 1][1] = lds_r128(vn + 512);
        }
        SB();
        v4u p0, p1; pexp(sc, p0, p1);
        o = MFMA32(asfrag(vf[kt & 1][0]), asfrag(p0), o); o = MFMA32(asfrag(vf[kt & 1][1]), asfrag(p1), o);
        SB();
        sc = sn;
      }
    } else {
      {
        f32x16 s8 = MFMA32(asfrag(lds_r128(kbase + 4 * U + 4 * U)), qf, zero);
#pragma unroll
        for (int r = 0; r < 16; r++) { const int kc = (r & 3) + 8 * (r >> 2); sm[r] = (qq >= kc) ? s8[r] : -3.0e38f; }
        m = tmax(sm, -3.0e38f);
      }
#pragma unroll 1
      for (int kt = nskip; kt < 8; kt++) { f32x16 s = MFMA32(asfrag(lds_r128(kbase + kt * U)), qf, zero); m = tmax(s, m); }
      { auto rr = __builtin_amdgcn_permlane32_swap(__float_as_uint(m), __float_as_uint(m), false, false); m = __builtin_fmaxf(__uint_as_float(rr[0]), __uint_as_float(rr[1])); }
      f32x16 negm;
#pragma unroll
      for (int r = 0; r < 16; r++) { negm[r] = -m; sm[r] -= m; }
      {
        v4u p0, p1; pexp(sm, p0, p1);
        ldsp v8 = va + 8 * vstride;
        o = MFMA32(asfrag(lds_r128(v8)), asfrag(p0), o); o = MFMA32(asfrag(lds_r128(v8 + 512)), asfrag(p1), o);
      }
#pragma unroll 1
      for (int kt = nskip; kt < 8; kt++) {
        f32x16 s = MFMA32(asfrag(lds_r128(kbase + kt * U)), qf, negm);
        v4u p0, p1; pexp(s, p0, p1);
        ldsp vn = va + kt * vstride;
        o = MFMA32(asfrag(lds_r128(vn)), asfrag(p0), o); o = MFMA32(asfrag(lds_r128(vn + 512)), asfrag(p1), o);
      }
    }
    float inv = __builtin_amdgcn_rcpf(o[8]);
    v4u ov;
#pragma unroll
    for (int j = 0; j < 4; j++) ov[j] = pks(o[2 * j] * inv, o[2 * j + 1] * inv);
    STAMP(6);
    GSTORE_SC1(ov, rws, vo_o, so_o);
    { unsigned z = 0, sh = HN_O + (unsigned)(ti * 64 + cg * 8 + w) * 4; ST32_SC1(htag, rws, z, sh); }
  }
  BAR_VM(2);
  STAMP(7);
  auto dunit = [&](int j) -> unsigned { return j < 8 ? 3 + j : (j < 10 ? j - 7 : (j == 10 ? 0 : (j < 19 ? j + 1 : j - 16))); };
  auto dissue = [&](int j) {
    unsigned dst = L0 + dunit(j) * U + w * 1024, so = (unsigned)((j * 8 + w) * 8192 + ch * 128);
    DMA(rwd, dst, dvoff, so, 0);
  };
  auto dissue_np = [&](int j0, int j1) {
    if (w != 0) {
#pragma unroll
      for (int j = j0; j < j1; j++) dissue(j);
    }
    if (w == 2) {
#pragma unroll
      for (int j = j0; j < j1; j++) { unsigned dst = L0 + dunit(j) * U, so = (unsigned)(j * 8 * 8192 + ch * 128); DMA(rwd, dst, dvoff, so, 0); }
    }
  };
  const int rt = w >> 1, kh = w & 1;
  v4u hv;
  {
    dissue_np(0, 8);
    const int row8 = 4 * (g & 1) + q4, swz8 = (row8 >> 1) & 3;
    bf16x8 wf[4][4];
    ldsp tb[4];
#pragma unroll
    for (int n = 0; n < 4; n++) tb[n] = L + 12 * U + kh * 32768 + (g >> 1) * 2048 + row8 * 128 + p4 * 8 + ((n ^ swz8) * 32);
    if (w == 0) wait_hint(rws, (unsigned)(l * 4), HN_O + (unsigned)tj * 256, htag);
    asm volatile("s_barrier" ::: "memory");
    STAMP(8);
    f32x4 acc[4];
#pragma unroll 1
    for (int spin = 0; spin < SPIN; ++spin) {
      v4u of[8];
#pragma unroll
      for (int k = 0; k < 8; k++) { unsigned so = WS_O + (unsigned)((tj * 4 + rt) * 16 + kh * 8 + k) * 1024; GLOAD_SC1(of[k], rws, v16, so); }
#pragma unroll
      for (int k = 0; k < 4; k++)
#pragma unroll
        for (int n = 0; n < 4; n++) wf[k][n] = trfrag(tb[n] + k * 4096, tb[n] + k * 4096 + 1024);
#pragma unroll
      for (int n = 0; n < 4; n++) acc[n] = (f32x4){0, 0, 0, 0};
      unsigned pm = 0;
      SB();
#define WO_STEP(k, N) asm volatile("s_waitcnt vmcnt(" #N ")" : "+v"(of[k])::"memory"); SB(); \
      _Pragma("unroll") for (int n = 0; n < 4; n++) acc[n] = MFMA16(wf[(k) & 3][n], asfrag(of[k]), acc[n]); pm = pchk(of[k], pm); SB(); \
      if ((k) < 4) { _Pragma("unroll") for (int n = 0; n < 4; n++) wf[(k) & 3][n] = trfrag(tb[n] + ((k) + 4) * 4096, tb[n] + ((k) + 4) * 4096 + 1024); } SB();
      WO_STEP(0, 7) WO_STEP(1, 6) WO_STEP(2, 5) WO_STEP(3, 4) WO_STEP(4, 3) WO_STEP(5, 2) WO_STEP(6, 1) WO_STEP(7, 0)
      if (!__any(pm == 0xffffffffu)) break;
    }
    {
      f32x4 s0 = kh ? acc[0] : acc[2], s1 = kh ? acc[1] : acc[3];
      lds_w128(L + 1 * U + w * 2048 + v16, __builtin_bit_cast(v4u, s0));
      lds_w128(L + 1 * U + w * 2048 + 1024 + v16, __builtin_bit_cast(v4u, s1));
    }
    const ldsp xa = L + (rt * 16 + c16) * 128 + kh * 64 + g * 8;
    v2u x0 = lds_r64(xa), x1 = lds_r64(xa + 32);
    BAR_LDS();
    {
      f32x4 r0 = asf(lds_r128(L + 1 * U + (w ^ 1) * 2048 + v16)), r1 = asf(lds_r128(L + 1 * U + (w ^ 1) * 2048 + 1024 + v16));
      f32x4 a0 = (kh ? acc[2] : acc[0]) + r0, a1 = (kh ? acc[3] : acc[1]) + r1;
      hv[0] = pks(lo16(x0[0]) + bfr(a0[0]), hi16(x0[0]) + bfr(a0[1])); hv[1] = pks(lo16(x0[1]) + bfr(a0[2]), hi16(x0[1]) + bfr(a0[3]));
      hv[2] = pks(lo16(x1[0]) + bfr(a1[0]), hi16(x1[0]) + bfr(a1[1])); hv[3] = pks(lo16(x1[1]) + bfr(a1[2]), hi16(x1[1]) + bfr(a1[3]));
      STAMP(9);
      GSTORE_SC1(hv, rws, v16, so_h);
      { unsigned z = 0, sh = HN_H + (unsigned)(tj * 64 + ch * 8 + w) * 4; ST32_SC1(htag, rws, z, sh); }
    }
  }
  v4u cp[4];
  {
    const unsigned cvo = (unsigned)(c16 * 64 + g * 16);
#pragma unroll
    for (int t = 0; t < 4; t++) {
      int tile = (2 * ks0 + w + ((t >> 1) && two ? 8 : 0)) * 2 + (t & 1);
      unsigned so = WS_CP + (unsigned)tile * 1024; GLOAD_SC1(cp[t], rws, cvo, so);
    }
    if (w != 0) {
#pragma unroll
      for (int n = P1; n < 32; n++) wload(n, wt0);
    }
    if (w == 0) wait_hint(rws, (unsigned)(l * 4), HN_H + (unsigned)tj * 256, htag);
    BAR_LDS();
    STAMP(10);
    fetch8k(rws, WS_H + (unsigned)((tj * 4 + rt) * 16 + kh * 8) * 1024, L, (12 + w) * U, L0, v16);
    STAMP(11);
    asm volatile("s_barrier" : "+v"(cp[0]), "+v"(cp[1]), "+v"(cp[2]), "+v"(cp[3])::"memory");
#pragma unroll
    for (int n = 0; n < P1; n += 4) asm volatile("" : "+v"(wq[n]), "+v"(wq[n + 1]), "+v"(wq[n + 2]), "+v"(wq[n + 3]));
    if (w == 0) {
#pragma unroll
      for (int n = P1; n < 32; n++) wload(n, wt0);
    }
    dissue(8); dissue(9); dissue(10);
  }
  f32x4 c4[4];
  STAMP(12);
  {
    const ldsp ab = L + 12 * U + v16;
    const ldsp abs_ = ab + (w >> 1) * 16384 + (w & 1) * 1024;
    const ldsp TB = L + 11 * U;
    float ri[4], mr[4];
    const unsigned avo = v16;
    auto pass = [&](auto PASS) {
      constexpr int ps = decltype(PASS)::value;
      const unsigned ht_ = htag, av_ = avo; const v4u rw_ = rws;
      f32x4 ag[4], au[4];
#pragma unroll
      for (int r = 0; r < 4; r++) { ag[r] = (f32x4){0, 0, 0, 0}; au[r] = (f32x4){0, 0, 0, 0}; }
      float s0 = 0.f, s1 = 0.f, q0 = 0.f, q1 = 0.f;
      bf16x8 ha[2][4];
      v4u hs[2];
      auto rd = [&](int ks) {
#pragma unroll
        for (int r = 0; r < 4; r++) ha[ks & 1][r] = asfrag(lds_r128(ab + (r * 16 + ks) * 1024));
        if (ps == 0 && (ks % 3) != 2 && ks < 12) hs[(ks - ks / 3) & 1] = lds_r128(abs_ + (ks - ks / 3) * 2048);
      };
      rd(0);
#pragma unroll
      for (int ks = 0; ks < 16; ks++) {
        const int b = ks & 1;
        const bool st = ps == 0 && (ks % 3) != 2 && ks < 12;
        const int sf = (ks - ks / 3) & 1;
        if (ps == 1) {
          switch (ks) {
#define WV(K, N) case K: asm volatile("s_waitcnt vmcnt(" #N ")" : "+v"(wq[2 * K]), "+v"(wq[2 * K + 1])::"memory"); break;
            WV(0, 30) WV(1, 28) WV(2, 26) WV(3, 24) WV(4, 22) WV(5, 20) WV(6, 18) WV(7, 16) WV(8, 14) WV(9, 12) WV(10, 10) WV(11, 8) WV(12, 6) WV(13, 4) WV(14, 2) WV(15, 0)
          }
        }
        if (ps == 0 && ks == 12) asm volatile("s_waitcnt vmcnt(27)" : "+v"(wq[24]), "+v"(wq[25]), "+v"(wq[26]), "+v"(wq[27]), "+v"(wq[28]), "+v"(wq[29]), "+v"(wq[30]), "+v"(wq[31])::"memory");
        if (ps == 0 && ks == 13) BAR_LDS();
        SB();
        ag[0] = MFMA16(asfrag(wq[2 * ks]), ha[b][0], ag[0]);
        SB();
        if (ks < 15) rd(ks + 1);
        SB();
        ag[1] = MFMA16(asfrag(wq[2 * ks]), ha[b][1], ag[1]);
        SB(); if (st) STAT(hs[sf][0], s0, s1, q0, q1); SB();
        ag[2] = MFMA16(asfrag(wq[2 * ks]), ha[b][2], ag[2]);
        ag[3] = MFMA16(asfrag(wq[2 * ks]), ha[b][3], ag[3]);
        SB(); if (st) STAT(hs[sf][1], s0, s1, q0, q1); SB();
        au[0] = MFMA16(asfrag(wq[2 * ks + 1]), ha[b][0], au[0]);
        au[1] = MFMA16(asfrag(wq[2 * ks + 1]), ha[b][1], au[1]);
        SB(); if (st) STAT(hs[sf][2], s0, s1, q0, q1); SB();
        au[2] = MFMA16(asfrag(wq[2 * ks + 1]), ha[b][2], au[2]);
        au[3] = MFMA16(asfrag(wq[2 * ks + 1]), ha[b][3], au[3]);
        SB(); if (st) STAT(hs[sf][3], s0, s1, q0, q1); SB();
        if (ps == 0 && two) { wload(2 * ks, wt1); wload(2 * ks + 1, wt1); }
        if (ps == 0 && ks == 12) {
          float ss = sum_g(s0 + s1), qq = sum_g(q0 + q1);
          if (g == 0) lds_w64(TB + 1024 + (w & 1) * 512 + ((w >> 1) * 16 + c16) * 8, (v2u){__float_as_uint(ss), __float_as_uint(qq)});
        }
        if (ps == 0 && ks == 13) {
          v2u a = lds_r64(TB + 1024 + l * 8), b2 = lds_r64(TB + 1024 + 512 + l * 8);
          float rinv, m_;
          ln_scalars(__uint_as_float(a[0]) + __uint_as_float(b2[0]), __uint_as_float(a[1]) + __uint_as_float(b2[1]), rinv, m_);
          lds_w64(TB + 4096 + w * 512 + l * 8, (v2u){__float_as_uint(rinv), __float_as_uint(m_)});
        }
        if (ps == 0 && ks == 14) {
#pragma unroll
          for (int r = 0; r < 4; r++) { v2u s = lds_r64(TB + 4096 + w * 512 + (r * 16 + c16) * 8); ri[r] = __uint_as_float(s[0]); mr[r] = __uint_as_float(s[1]); }
        }
        if (ps == 0 && (ks == 2 || ks == 5 || ks == 8 || ks == 11)) c4[ks / 3] = sum16x4(cp[ks / 3]);
        SB();
      }
      v2u ap[4];
#pragma unroll
      for (int r = 0; r < 4; r++) {
        f32x4 gg = (ag[r] * ri[r] - c4[2 * ps] * mr[r]) * -1.4426950408889634f, uu = au[r] * ri[r] - c4[2 * ps + 1] * mr[r];
        float a[4];
#pragma unroll
        for (int j = 0; j < 4; j++) a[j] = uu[j] * __builtin_amdgcn_rcpf(1.f + __builtin_amdgcn_exp2f(gg[j]));
        ap[r] = (v2u){pks(a[0], a[1]), pks(a[2], a[3])};
      }
      const int pg = 2 * ks0 + w + 8 * ps;
      v4u b0 = join16(ap[0], ap[1]), b1 = join16(ap[2], ap[3]);
      unsigned so = WS_ACT + (unsigned)(tj * 2 * 86 + pg) * 1024, s2 = so + 86 * 1024;
      GSTORE_SC1(b0, rw_, av_, so); GSTORE_SC1(b1, rw_, av_, s2);
      { unsigned z = 0, sh = HN_A + (unsigned)(tj * 128 + pg) * 4; ST32_SC1(ht_, rw_, z, sh); }
    };
    pass(std::integral_constant<int, 0>{});
    STAMP(13);
    if (two) pass(std::integral_constant<int, 1>{});
  }
  STAMP(14);
  {
    constexpr int NPRE = 7;
    int l5 = threadIdx.x & 63; asm volatile("" : "+v"(l5));
    const int l = l5, c16 = l & 15, g = l >> 4, q4 = (l >> 2) & 3, p4 = l & 3;
    const unsigned v16 = l * 16;
    const int swq = ((q4 >> 1) & 1) | ((g & 1) << 1);
    ldsp tb[4], tc[4], td[4];
#pragma unroll
    for (int n = 0; n < 4; n++) { tb[n] = L + kh * 4096 + g * 1024 + q4 * 128 + p4 * 8 + ((n ^ swq) * 32); tc[n] = tb[n] + 7 * U; td[n] = tb[n] + 14 * U; }
    bf16x8 wf[NPRE][4];
    auto rd = [&](int j) {
      const unsigned u = dunit(j);
#pragma unroll
      for (int n = 0; n < 4; n++) { const ldsp b = u < 7 ? tb[n] + u * U : (u < 14 ? tc[n] + (u - 7) * U : td[n] + (u - 14) * U); wf[j % NPRE][n] = trfrag(b, b + 512); }
    };
    BAR_VM(3);
    dissue_np(11, 19);
#pragma unroll
    for (int j = 0; j < NPRE; j++) rd(j);
    if (w == 0) {
      unsigned v0 = (unsigned)(l * 4), v1 = (unsigned)((l < 22 ? 64 + l : l) * 4);
      wait_hint(rws, v0, HN_A + (unsigned)tj * 512, htag);
      wait_hint(rws, v1, HN_A + (unsigned)tj * 512, htag);
    }
    BAR_LDS();
    STAMP(15);
    const unsigned avo = (unsigned)((g >> 1) * 1024 + (g & 1) * 512 + ((rt & 1) * 16 + c16) * 16);
    const unsigned aso = WS_ACT + (unsigned)((tj * 2 + (rt >> 1)) * 86 + 2 * kh) * 1024;
    const unsigned aso21 = aso + (unsigned)(kh ? 20 : 21) * 4096;
    v4u af[22];
#pragma unroll
    for (int j = 0; j < 22; j++) { unsigned so = j == 21 ? aso21 : aso + (unsigned)j * 4096; GLOAD_SC1(af[j], rws, avo, so); }
    dissue(19); dissue(20); dissue(21);
    f32x4 acc[4];
#pragma unroll
    for (int n = 0; n < 4; n++) acc[n] = (f32x4){0, 0, 0, 0};
#define DN_STEP(j, N) \
    asm volatile("s_waitcnt vmcnt(" #N ")" : "+v"(af[j])::"memory"); \
    if (__builtin_expect(__any(pchk(af[j], 0u) == 0xffffffffu), 0)) { \
      _Pragma("unroll 1") for (int spin = 0; spin < SPIN; ++spin) { \
        unsigned so = (j) == 21 ? aso21 : aso + (unsigned)(j) * 4096; \
        asm volatile("buffer_load_dwordx4 %0, %1, %2, %3 offen sc1\n\ts_waitcnt vmcnt(0)" : "=&v"(af[j]) : "v"(avo), "s"(rws), "s"(so) : "memory"); \
        if (!__any(pchk(af[j], 0u) == 0xffffffffu)) break; \
      } \
    } \
    SB(); \
    if ((j) < 21 || kh == 0) { _Pragma("unroll") for (int n = 0; n < 4; n++) acc[n] = MFMA16(wf[(j) % NPRE][n], asfrag(af[j]), acc[n]); } \
    SB(); \
    if ((j) + NPRE < 22) rd((j) + NPRE); \
    SB();
    DN_STEP(0, 24) DN_STEP(1, 23) DN_STEP(2, 22) DN_STEP(3, 21)
    asm volatile("s_barrier" ::: "memory");
    DN_STEP(4, 20) DN_STEP(5, 19) DN_STEP(6, 18) DN_STEP(7, 17) DN_STEP(8, 16) DN_STEP(9, 15) DN_STEP(10, 14) DN_STEP(11, 13)
    asm volatile("s_waitcnt vmcnt(0)\n\ts_barrier" : "+v"(af[12]), "+v"(af[13]), "+v"(af[14]), "+v"(af[15]), "+v"(af[16]), "+v"(af[17]), "+v"(af[18]), "+v"(af[19]),
                 "+v"(af[20]), "+v"(af[21])::"memory");
    DN_STEP(12, 0) DN_STEP(13, 0) DN_STEP(14, 0) DN_STEP(15, 0) DN_STEP(16, 0) DN_STEP(17, 0) DN_STEP(18, 0) DN_STEP(19, 0) DN_STEP(20, 0) DN_STEP(21, 0)
    STAMP(16);
    {
      f32x4 s0 = kh ? acc[0] : acc[2], s1 = kh ? acc[1] : acc[3];
      lds_w128(L + 1 * U + w * 2048 + v16, __builtin_bit_cast(v4u, s0));
      lds_w128(L + 1 * U + w * 2048 + 1024 + v16, __builtin_bit_cast(v4u, s1));
    }
    BAR_LDS();
    {
      f32x4 r0 = asf(lds_r128(L + 1 * U + (w ^ 1) * 2048 + v16)), r1 = asf(lds_r128(L + 1 * U + (w ^ 1) * 2048 + 1024 + v16));
      f32x4 a0 = (kh ? acc[2] : acc[0]) + r0, a1 = (kh ? acc[3] : acc[1]) + r1;
      v2u o0 = {pk(lo16(hv[0]) + bfr(a0[0]), hi16(hv[0]) + bfr(a0[1])), pk(lo16(hv[1]) + bfr(a0[2]), hi16(hv[1]) + bfr(a0[3]))};
      v2u o1 = {pk(lo16(hv[2]) + bfr(a1[0]), hi16(hv[2]) + bfr(a1[1])), pk(lo16(hv[3]) + bfr(a1[2]), hi16(hv[3]) + bfr(a1[3]))};
      v4u ov = join16(o0, o1);
      unsigned vo = (unsigned)(c16 * 1024 + (g & 1) * 32 + (g >> 1) * 16), so = (unsigned)((tj * 64 + rt * 16) * 1024 + ch * 128 + kh * 64);
      GSTORE(ov, rout, vo, so);
    }
  }
}

extern "C" void kernel_launch(void* const* d_in, const int* in_sizes, int n_in, void* d_out, int out_size, void* d_ws, size_t ws_size,
                              hipStream_t stream) {
  (void)hipFuncSetAttribute((const void*)fused_block, hipFuncAttributeMaxDynamicSharedMemorySize, LDS_BYTES);
  fused_block<<<256, 512, LDS_BYTES, stream>>>(d_in[0], d_in[1], d_in[2], d_in[4], d_in[5], d_in[6], d_ws, d_out);
}
```

```cpp
#define CODE_PF
#include <hip/hip_runtime.h>
#include <hip/hip_bf16.h>
#include <type_traits>

typedef unsigned v4u __attribute__((ext_vector_type(4)));
typedef unsigned v2u __attribute__((ext_vector_type(2)));
typedef float f32x2 __attribute__((ext_vector_type(2)));
typedef float f32x4 __attribute__((ext_vector_type(4)));
typedef float f32x16 __attribute__((ext_vector_type(16)));
typedef short bf16x8 __attribute__((ext_vector_type(8)));
typedef short s16x4 __attribute__((ext_vector_type(4)));
typedef __bf16 bf16x2_t __attribute__((ext_vector_type(2)));
#define LAS __attribute__((address_space(3)))
#define DI __device__ __forceinline__
#define MAPB 1

constexpr int S_ = 2048, D_ = 512, F_ = 1376;
constexpr unsigned WS_KV = 0;
constexpr unsigned WS_O = WS_KV + (6u << 20);
constexpr unsigned WS_H = WS_O + (2u << 20);
constexpr unsigned WS_ACT = WS_H + (2u << 20);
constexpr unsigned WS_WT = 16u << 20;
constexpr unsigned WS_CP = 19u << 20;
constexpr unsigned WS_FL = 20u << 20;
constexpr unsigned FL_0 = WS_FL;
constexpr unsigned HN_K = FL_0 + 4096;
constexpr unsigned HN_O = HN_K + 8192;
constexpr unsigned HN_H = HN_O + 8192;
constexpr unsigned HN_A = HN_H + 8192;
constexpr unsigned WS_EP = HN_A + 16384;
constexpr unsigned WS_END = WS_EP + 256;
constexpr int LDS_BYTES = 160 * 1024;
constexpr unsigned U = 8192;
constexpr unsigned OOB = 0x7ffffff0u;

DI v4u mk_rsrc(const void* p, unsigned bytes) {
  unsigned long long a = (unsigned long long)p;
  v4u r; r.x = (unsigned)a; r.y = (unsigned)(a >> 32) & 0xffffu; r.z = bytes; r.w = 0x00020000u; return r;
}
DI unsigned pk(float a, float b) { f32x2 v = {a, b}; bf16x2_t r = __builtin_convertvector(v, bf16x2_t); return __builtin_bit_cast(unsigned, r); }
DI unsigned pks(float a, float b) { unsigned u = pk(a, b); return u > 0xfffffffeu ? 0xfffffffeu : u; }
DI float bfr(float a) { return __uint_as_float(pk(a, a) << 16); }
DI float lo16(unsigned u) { return __uint_as_float(u << 16); }
DI float hi16(unsigned u) { return __uint_as_float(u & 0xffff0000u); }
DI float dot_one(unsigned u, float c) { return __builtin_amdgcn_fdot2_f32_bf16(__builtin_bit_cast(bf16x2_t, u), __builtin_bit_cast(bf16x2_t, 0x3f803f80u), c, false); }
DI unsigned umax3(unsigned a, unsigned b, unsigned c) { unsigned m = a > b ? a : b; return m > c ? m : c; }
DI unsigned pchk(v4u v, unsigned m) { return umax3(umax3(m, v[0], v[1]), v[2], v[3]); }

#define DMA(rsrc, lds_addr, voff, soff, OFF) \
  asm volatile("s_mov_b32 m0, %0\n\ts_nop 0\n\tbuffer_load_dwordx4 %1, %2, %3 offen offset:" #OFF " lds" ::"s"(lds_addr), "v"(voff), "s"(rsrc), "s"(soff) : "memory")
#define DMA_SC1(rsrc, lds_addr, voff, soff, OFF) \
  asm volatile("s_mov_b32 m0, %0\n\ts_nop 0\n\tbuffer_load_dwordx4 %1, %2, %3 offen offset:" #OFF " sc1 lds" ::"s"(lds_addr), "v"(voff), "s"(rsrc), "s"(soff) : "memory")
#define GLOAD_SC1(dst, rsrc, voff, soff) \
  asm volatile("buffer_load_dwordx4 %0, %1, %2, %3 offen sc1" : "=v"(dst) : "v"(voff), "s"(rsrc), "s"(soff) : "memory")
#define GSTORE(val, rsrc, voff, soff) \
  asm volatile("buffer_store_dwordx4 %0, %1, %2, %3 offen\n\ts_nop 1" ::"v"(val), "v"(voff), "s"(rsrc), "s"(soff) : "memory")
#define GSTORE_SC1(val, rsrc, voff, soff) \
  asm volatile("buffer_store_dwordx4 %0, %1, %2, %3 offen sc1\n\ts_nop 1" ::"v"(val), "v"(voff), "s"(rsrc), "s"(soff) : "memory")
#define ST64_SC1(val, rsrc, voff, soff) \
  asm volatile("buffer_store_dwordx2 %0, %1, %2, %3 offen sc1\n\ts_nop 1" ::"v"(val), "v"(voff), "s"(rsrc), "s"(soff) : "memory")
#define ST32_SC1(val, rsrc, voff, soff) \
  asm volatile("buffer_store_dword %0, %1, %2, %3 offen sc1" ::"v"(val), "v"(voff), "s"(rsrc), "s"(soff) : "memory")
#define WAIT_VM(N) asm volatile("s_waitcnt vmcnt(" #N ")" ::: "memory")
#define BAR_VM(N) asm volatile("s_waitcnt vmcnt(" #N ") lgkmcnt(0)\n\ts_barrier" ::: "memory")
#define BAR_LDS() asm volatile("s_waitcnt lgkmcnt(0)\n\ts_barrier" ::: "memory")
#define SB() __builtin_amdgcn_sched_barrier(0)
#define MFMA16(a, b, c) __builtin_amdgcn_mfma_f32_16x16x32_bf16((a), (b), (c), 0, 0, 0)
#define MFMA32(a, b, c) __builtin_amdgcn_mfma_f32_32x32x16_bf16((a), (b), (c), 0, 0, 0)
#define STAT(u, s0, s1, q0, q1) do { float lo_, hi_; \
  asm volatile("v_lshlrev_b32 %0, 16, %6\n\tv_and_b32 %1, 0xffff0000, %6\n\tv_add_f32 %2, %2, %0\n\tv_add_f32 %3, %3, %1\n\tv_fmac_f32 %4, %0, %0\n\tv_fmac_f32 %5, %1, %1" \
               : "=&v"(lo_), "=&v"(hi_), "+v"(s0), "+v"(s1), "+v"(q0), "+v"(q1) : "v"(u)); } while (0)

typedef LAS char* ldsp;
DI s16x4 trd(ldsp a) { return __builtin_amdgcn_ds_read_tr16_b64_v4i16((LAS s16x4*)a); }
DI bf16x8 trfrag(ldsp a0, ldsp a1) { s16x4 lo = trd(a0), hi = trd(a1); return __builtin_shufflevector(lo, hi, 0, 1, 2, 3, 4, 5, 6, 7); }
DI v4u lds_r128(ldsp a) { return *(LAS v4u*)a; }
DI void lds_w128(ldsp a, v4u v) { *(LAS v4u*)a = v; }
DI void lds_w64(ldsp a, v2u v) { *(LAS v2u*)a = v; }
DI v2u lds_r64(ldsp a) { return *(LAS v2u*)a; }
DI bf16x8 asfrag(v4u v) { return __builtin_bit_cast(bf16x8, v); }
DI v4u asu(bf16x8 v) { return __builtin_bit_cast(v4u, v); }
DI f32x4 asf(v4u v) { return __builtin_bit_cast(f32x4, v); }

template <int CTRL> DI float dpp_add(float v) {
  int t = __builtin_amdgcn_update_dpp(0, __float_as_int(v), CTRL, 0xf, 0xf, false);
  return v + __int_as_float(t);
}
DI float sum16(float v) { v = dpp_add<0x128>(v); v = dpp_add<0x124>(v); v = dpp_add<0x122>(v); v = dpp_add<0x121>(v); return v; }
DI f32x4 sum16x4(v4u v) {
  asm volatile("s_nop 1\n\t"
    "v_add_f32_dpp %0, %0, %0 row_ror:8 row_mask:0xf bank_mask:0xf\n\tv_add_f32_dpp %1, %1, %1 row_ror:8 row_mask:0xf bank_mask:0xf\n\t"
    "v_add_f32_dpp %2, %2, %2 row_ror:8 row_mask:0xf bank_mask:0xf\n\tv_add_f32_dpp %3, %3, %3 row_ror:8 row_mask:0xf bank_mask:0xf\n\t"
    "v_add_f32_dpp %0, %0, %0 row_ror:4 row_mask:0xf bank_mask:0xf\n\tv_add_f32_dpp %1, %1, %1 row_ror:4 row_mask:0xf bank_mask:0xf\n\t"
    "v_add_f32_dpp %2, %2, %2 row_ror:4 row_mask:0xf bank_mask:0xf\n\tv_add_f32_dpp %3, %3, %3 row_ror:4 row_mask:0xf bank_mask:0xf\n\t"
    "v_add_f32_dpp %0, %0, %0 row_ror:2 row_mask:0xf bank_mask:0xf\n\tv_add_f32_dpp %1, %1, %1 row_ror:2 row_mask:0xf bank_mask:0xf\n\t"
    "v_add_f32_dpp %2, %2, %2 row_ror:2 row_mask:0xf bank_mask:0xf\n\tv_add_f32_dpp %3, %3, %3 row_ror:2 row_mask:0xf bank_mask:0xf\n\t"
    "v_add_f32_dpp %0, %0, %0 row_ror:1 row_mask:0xf bank_mask:0xf\n\tv_add_f32_dpp %1, %1, %1 row_ror:1 row_mask:0xf bank_mask:0xf\n\t"
    "v_add_f32_dpp %2, %2, %2 row_ror:1 row_mask:0xf bank_mask:0xf\n\tv_add_f32_dpp %3, %3, %3 row_ror:1 row_mask:0xf bank_mask:0xf"
    : "+v"(v[0]), "+v"(v[1]), "+v"(v[2]), "+v"(v[3]));
  return __builtin_bit_cast(f32x4, v);
}
DI float swap32_add(float a, float b) { auto r = __builtin_amdgcn_permlane32_swap(__float_as_uint(a), __float_as_uint(b), false, false); return __uint_as_float(r[0]) + __uint_as_float(r[1]); }
DI float swap16_add(float a, float b) { auto r = __builtin_amdgcn_permlane16_swap(__float_as_uint(a), __float_as_uint(b), false, false); return __uint_as_float(r[0]) + __uint_as_float(r[1]); }
DI float sum_g(float v) { v = swap16_add(v, v); return swap32_add(v, v); }
DI v4u join16(v2u a, v2u b) {
  auto r0 = __builtin_amdgcn_permlane16_swap(a[0], b[0], false, false), r1 = __builtin_amdgcn_permlane16_swap(a[1], b[1], false, false);
  return (v4u){r0[0], r1[0], r0[1], r1[1]};
}
DI v4u join32(v2u a, v2u b) {
  auto r0 = __builtin_amdgcn_permlane32_swap(a[0], b[0], false, false), r1 = __builtin_amdgcn_permlane32_swap(a[1], b[1], false, false);
  return (v4u){r0[0], r1[0], r0[1], r1[1]};
}
DI void ln_scalars(float s, float q, float& rinv, float& mr) {
  float mean = s * (1.f / 512.f);
  float var = __builtin_fmaxf(__builtin_fmaf(-mean, mean, q * (1.f / 512.f)), 0.f);
  float mb = bfr(mean), vb = bfr(var), t = bfr(vb + 1e-5f), sd = bfr(__builtin_amdgcn_sqrtf(t));
  rinv = __builtin_amdgcn_rcpf(sd); mr = mb * rinv;
}
DI float mx3(float a, float b, float c) { return __builtin_fmaxf(__builtin_fmaxf(a, b), c); }
DI float tmax(const f32x16& s, float m) {
#pragma unroll
  for (int r = 0; r < 16; r += 2) m = mx3(m, s[r], s[r + 1]);
  return m;
}
DI void pexp(const f32x16& s, v4u& p0, v4u& p1) {
#pragma unroll
  for (int j = 0; j < 4; j++) {
    p0[j] = pk(__builtin_amdgcn_exp2f(s[2 * j]), __builtin_amdgcn_exp2f(s[2 * j + 1]));
    p1[j] = pk(__builtin_amdgcn_exp2f(s[8 + 2 * j]), __builtin_amdgcn_exp2f(s[8 + 2 * j + 1]));
  }
}

#define STAMP(idx)
constexpr int SPIN = 40000;

DI void wait_hint(v4u rws, unsigned voff, unsigned soff, unsigned tag) {
#pragma unroll 1
  for (int spin = 0; spin < SPIN; ++spin) {
    unsigned f;
    asm volatile("buffer_load_dword %0, %1, %2, %3 offen sc1\n\ts_waitcnt vmcnt(0)" : "=&v"(f) : "v"(voff), "s"(rws), "s"(soff) : "memory");
    if (__all(f == tag)) break;
    __builtin_amdgcn_s_sleep(2);
  }
}
DI void fetch8k(v4u rws, unsigned so, ldsp L, unsigned dst, unsigned L0, unsigned v16) {
#pragma unroll 1
  for (int spin = 0; spin < SPIN; ++spin) {
    unsigned s2 = so, d2 = L0 + dst;
    DMA_SC1(rws, d2, v16, s2, 0); DMA_SC1(rws, d2, v16, s2, 1024); DMA_SC1(rws, d2, v16, s2, 2048); DMA_SC1(rws, d2, v16, s2, 3072);
    s2 += 4096; d2 += 4096;
    DMA_SC1(rws, d2, v16, s2, 0); DMA_SC1(rws, d2, v16, s2, 1024); DMA_SC1(rws, d2, v16, s2, 2048); DMA_SC1(rws, d2, v16, s2, 3072);
    WAIT_VM(0);
    unsigned m = 0;
#pragma unroll
    for (int k = 0; k < 8; k++) m = pchk(lds_r128(L + dst + v16 + k * 1024), m);
    if (!__any(m == 0xffffffffu)) break;
  }
}

constexpr int P1 = 24;

__global__ __launch_bounds__(512, 2) __attribute__((amdgpu_waves_per_eu(2, 2)))
void fused_block(const void* x, const void* wqkv, const void* wo, const void* wg, const void* wu, const void* wd, void* ws, void* out) {
  extern __shared__ __attribute__((aligned(16))) char smem[];
  const ldsp L = (ldsp)smem;
  const unsigned L0 = (unsigned)(size_t)L;
  const int bid = blockIdx.x;
  const int ti = ((bid & 7) >> 1) * 8 + (bid >> 5), cg = (bid & 1) * 4 + ((bid >> 3) & 3);
  const int tj = MAPB ? (bid & 7) * 4 + (bid >> 6) : ti, ch = MAPB ? (bid >> 3) & 7 : cg;
  const int tid = threadIdx.x, l = tid & 63, w = __builtin_amdgcn_readfirstlane(tid >> 6);
  const int c16 = l & 15, g = l >> 4, q4 = (l >> 2) & 3, p4 = l & 3;
  const int gw = bid * 8 + w;
  const v4u rx = mk_rsrc(x, S_ * D_ * 2), rwq = mk_rsrc(wqkv, D_ * 1536 * 2), rwo = mk_rsrc(wo, D_ * D_ * 2);
  const v4u rgu = mk_rsrc(((gw >> 4) & 1) ? wu : wg, D_ * F_ * 2), rwd = mk_rsrc(wd, F_ * D_ * 2);
  const v4u rws = mk_rsrc(ws, WS_END), rout = mk_rsrc(out, S_ * D_ * 2);
  const unsigned v16 = l * 16;
  const int rg = w >> 2, hd = w & 3;
  const int nks = ch < 3 ? 6 : 5, ks0 = ch < 3 ? 6 * ch : 18 + 5 * (ch - 3);
  STAMP(0);
  {
    unsigned long long pc; asm volatile("s_getpc_b64 %0" : "=s"(pc));
    const char* cp_ = (const char*)(pc & ~127ull) + (size_t)(w * 64 + l) * 128;
    unsigned dst = L0 + 19 * U + w * 256;
    if (w < 5) asm volatile("s_mov_b32 m0, %0\n\ts_nop 0\n\tglobal_load_lds_dword %1, off" ::"s"(dst), "v"(cp_) : "memory");
  }
  v2u ep;
  { unsigned z = 0, so = WS_EP; asm volatile("buffer_load_dwordx2 %0, %1, %2, %3 offen sc1" : "=v"(ep) : "v"(z), "s"(rws), "s"(so) : "memory"); }
  const bool do_pt = gw < 1376;
  const unsigned PTS = 17 * U + w * 2048;
  if (do_pt) {
    unsigned vo = (l >> 2) * 2752 + (l & 3) * 16, so = (unsigned)((gw & 15) * 32 * 2752 + (gw >> 5) * 64), dst = L0 + PTS;
    DMA(rgu, dst, vo, so, 0);
    so += 16 * 2752; dst += 1024;
    DMA(rgu, dst, vo, so, 0);
  }
  const int kb = w & 3, ksl = w >> 2;
  unsigned qvoff, xvoff;
  { int row = l >> 3, c = l & 7; int swz = ((row >> 1) & 1) | ((kb & 1) << 1); qvoff = row * 3072 + ((c ^ (swz << 1)) * 16);
    int r16 = (w & 1) * 8 + row; xvoff = row * 1024 + ((c ^ ((r16 >> 1) & 7)) * 16); }
  auto xissue = [&](int c) {
    unsigned dst = L0 + c * U + w * 1024, so = (unsigned)((ti * 64 + w * 8) * 1024 + c * 128);
    DMA(rx, dst, xvoff, so, 0);
  };
  auto qissue = [&](int c) {
    unsigned dst = L0 + 8 * U + ((2 * c) & 7) * 12288 + ksl * 12288 + kb * 3072;
    unsigned so = (unsigned)((32 * (2 * c + ksl) + 8 * kb) * 3072 + cg * 128);
    DMA(rwq, dst, qvoff, so, 0); DMA(rwq, dst, qvoff, so, 1024); DMA(rwq, dst, qvoff, so, 2048);
  };
  xissue(0); qissue(0); xissue(1); qissue(1); xissue(2); qissue(2); xissue(3); xissue(4); xissue(5); xissue(6); xissue(7);
  const unsigned so_kv = WS_KV + (unsigned)((ti * 2 + rg) * 32 + cg * 4 + hd) * 2048;
  const unsigned so_o = WS_O + (unsigned)((ti * 4 + rg * 2) * 16 + cg * 2 + (hd >> 1)) * 1024;
  const unsigned vo_o = (unsigned)(((l & 31) >> 4) * 16384 + ((2 * (hd & 1) + (l >> 5)) * 16 + c16) * 16);
  const unsigned so_h = WS_H + (unsigned)((tj * 4 + (w >> 1)) * 16 + ch * 2 + (w & 1)) * 1024;
  {
    const v4u PZ = {0xffffffffu, 0xffffffffu, 0xffffffffu, 0xffffffffu};
    unsigned s2 = so_kv + 1024;
    GSTORE_SC1(PZ, rws, v16, so_kv); GSTORE_SC1(PZ, rws, v16, s2); GSTORE_SC1(PZ, rws, vo_o, so_o); GSTORE_SC1(PZ, rws, v16, so_h);
#pragma unroll
    for (int b = 0; b < 3; b++) {
      int idx = 3 * w + b, tg = idx >= 12 ? 1 : 0, p = idx - 12 * tg;
      unsigned so = p < 2 * nks ? WS_ACT + (unsigned)((tj * 2 + tg) * 86 + 2 * ks0 + p) * 1024 : OOB;
      GSTORE_SC1(PZ, rws, v16, so);
    }
  }
  asm volatile("s_waitcnt vmcnt(24)" : "+v"(ep)::"memory");
  const unsigned long long epoch = ((unsigned long long)(unsigned)__builtin_amdgcn_readfirstlane(ep[1]) << 32) | (unsigned)__builtin_amdgcn_readfirstlane(ep[0]);
  v2u tag;
  { unsigned long long e = epoch * 0x9E3779B97F4A7C15ull + 0x632BE59BD9B4E019ull; tag[0] = (unsigned)e; tag[1] = (unsigned)(e >> 32); }
  const unsigned htag = tag[1];
  if (do_pt) {
    const ldsp a = L + PTS + (4 * g + q4) * 64 + p4 * 8;
    v4u f0 = asu(trfrag(a, a + 1024)), f1 = asu(trfrag(a + 32, a + 32 + 1024));
    const unsigned t0 = (unsigned)((gw >> 5) * 4 + ((gw >> 4) & 1));
    unsigned so = WS_WT + (t0 * 16 + (gw & 15)) * 1024;
    GSTORE_SC1(f0, rws, v16, so);
    so += 32 * 1024;
    GSTORE_SC1(f1, rws, v16, so);
    float c0 = 0.f, c1 = 0.f;
#pragma unroll
    for (int j = 0; j < 4; j++) { c0 = dot_one(f0[j], c0); c1 = dot_one(f1[j], c1); }
    c0 = sum_g(c0); c1 = sum_g(c1);
    unsigned cv = __float_as_uint(g == 0 ? c0 : c1), vo = g < 2 ? (unsigned)(g * 2048 + c16 * 4) : OOB;
    unsigned sc = WS_CP + (t0 * 16 + (gw & 15)) * 64;
    ST32_SC1(cv, rws, vo, sc);
  }
  STAMP(1);
  bf16x8 qf;
  {
    f32x4 acc[3][2], cs[3];
#pragma unroll
    for (int s = 0; s < 3; s++) { acc[s][0] = (f32x4){0, 0, 0, 0}; acc[s][1] = (f32x4){0, 0, 0, 0}; cs[s] = (f32x4){0, 0, 0, 0}; }
    const bf16x8 ones = asfrag((v4u){0x3f803f80u, 0x3f803f80u, 0x3f803f80u, 0x3f803f80u});
    float s0 = 0.f, s1 = 0.f, q0 = 0.f, q1 = 0.f;
    ldsp tb0, tb1;
    { int swz = ((q4 >> 1) & 1) | ((g & 1) << 1); tb0 = L + 8 * U + g * 3072 + q4 * 128 + ((hd ^ swz) * 32) + p4 * 8; tb1 = tb0 + 49152; }
    const unsigned xo0 = (c16 >> 3) * 1024 + (c16 & 7) * 128 + ((g ^ (c16 >> 1)) * 16), xo1 = xo0 ^ 64;
    const ldsp xb0 = L + rg * 4096 + xo0, xb1 = L + rg * 4096 + xo1;
    const ldsp xbs = L + (w >> 1) * 2048 + ((w & 1) ? xo1 : xo0);
    const ldsp TB = L + 8 * U;
    bf16x8 wf[2][3], xa[2][2];
    v4u xs[2];
    auto rd = [&](int ks) {
      const int sl = ks & 7, b = ks & 1;
      const ldsp t = sl < 4 ? tb0 + sl * 12288 : tb1 + (sl - 4) * 12288;
      const ldsp xp = (b ? xb1 : xb0) + (ks >> 1) * U;
      xa[b][0] = asfrag(lds_r128(xp)); xa[b][1] = asfrag(lds_r128(xp + 2048));
#pragma unroll
      for (int s = 0; s < 3; s++) wf[b][s] = trfrag(t + s * 1024, t + s * 1024 + 512);
    };
#pragma unroll
    for (int ks = 0; ks < 16; ks++) {
      const int b = ks & 1;
      const bool st = ks >= 5 && ks <= 12;
      if (ks == 0) { BAR_VM(16); rd(0); }
      if (ks == 2) BAR_VM(15);
      if (ks == 4 || ks == 6 || ks == 8 || ks == 10) BAR_VM(3);
      if (ks == 12) BAR_VM(0);
      if (ks == 14) BAR_LDS();
      SB();
      acc[0][0] = MFMA16(wf[b][0], xa[b][0], acc[0][0]);
      SB();
      if (ks < 15) rd(ks + 1);
      if (ks >= 4 && ks <= 11) xs[b ^ 1] = lds_r128(xbs + (ks - 4) * U);
      SB();
      acc[0][1] = MFMA16(wf[b][0], xa[b][1], acc[0][1]);
      SB();
      if (b == 0 && ks <= 8) qissue(ks / 2 + 3);
      if (ks == 4 && w == 0) { unsigned z = 0, so = FL_0 + (unsigned)bid * 8; ST64_SC1(tag, rws, z, so); }
      SB();
      acc[1][0] = MFMA16(wf[b][1], xa[b][0], acc[1][0]);
      acc[1][1] = MFMA16(wf[b][1], xa[b][1], acc[1][1]);
      SB(); if (st) STAT(xs[b][0], s0, s1, q0, q1); SB();
      acc[2][0] = MFMA16(xa[b][0], wf[b][2], acc[2][0]);
      acc[2][1] = MFMA16(xa[b][1], wf[b][2], acc[2][1]);
      SB(); if (st) STAT(xs[b][1], s0, s1, q0, q1); SB();
      cs[0] = MFMA16(wf[b][0], ones, cs[0]);
      cs[1] = MFMA16(wf[b][1], ones, cs[1]);
      SB(); if (st) STAT(xs[b][2], s0, s1, q0, q1); SB();
      cs[2] = MFMA16(ones, wf[b][2], cs[2]);
      SB(); if (st) STAT(xs[b][3], s0, s1, q0, q1); SB();
      if (ks == 13) {
        float ss = sum_g(s0 + s1), qq = sum_g(q0 + q1);
        if (g == 0) lds_w64(TB + (w & 1) * 512 + ((w >> 1) * 16 + c16) * 8, (v2u){__float_as_uint(ss), __float_as_uint(qq)});
      }
      if (ks == 14) {
        const int tok = rg * 32 + (g & 1) * 16 + c16;
        v2u a = lds_r64(TB + tok * 8), b2 = lds_r64(TB + 512 + tok * 8);
        float rinv, mr;
        ln_scalars(__uint_as_float(a[0]) + __uint_as_float(b2[0]), __uint_as_float(a[1]) + __uint_as_float(b2[1]), rinv, mr);
        if (g < 2) lds_w64(TB + 4096 + w * 256 + (g * 16 + c16) * 8, (v2u){__float_as_uint(rinv), __float_as_uint(mr)});
      }
    }
    STAMP(2);
    const float QS = 0.25f * 1.4426950408889634f;
    v2u qp[2], kp[2], vp[2];
#pragma unroll
    for (int rt = 0; rt < 2; rt++) {
      v2u sc = lds_r64(TB + 4096 + w * 256 + (rt * 16 + c16) * 8);
      f32x4 s01 = asf(lds_r128(TB + 4096 + w * 256 + (rt * 16 + 4 * g) * 8)), s23 = asf(lds_r128(TB + 4096 + w * 256 + (rt * 16 + 4 * g) * 8 + 16));
      const float ri = __uint_as_float(sc[0]), mr = __uint_as_float(sc[1]);
      f32x4 kv = acc[1][rt] * ri - cs[1] * mr, qv = (acc[0][rt] * ri - cs[0] * mr) * QS;
      kp[rt] = (v2u){pks(kv[0], kv[1]), pks(kv[2], kv[3])};
      qp[rt] = (v2u){pk(qv[0], qv[1]), pk(qv[2], qv[3])};
      const float cv = cs[2][0];
      float v0 = acc[2][rt][0] * s01[0] - cv * s01[1], v1 = acc[2][rt][1] * s01[2] - cv * s01[3];
      float v2 = acc[2][rt][2] * s23[0] - cv * s23[1], v3 = acc[2][rt][3] * s23[2] - cv * s23[3];
      vp[rt] = (v2u){pks(v0, v1), pks(v2, v3)};
    }
    v4u kk = join16(kp[0], kp[1]), vv = join32(vp[0], vp[1]);
    { unsigned s2 = so_kv + 1024; GSTORE_SC1(kk, rws, v16, so_kv); GSTORE_SC1(vv, rws, v16, s2);
      unsigned z = 0, sh = HN_K + (unsigned)((cg * 64 + ti * 2 + rg) * 4 + hd) * 4; ST32_SC1(htag, rws, z, sh); }
    const ldsp kvs = L + (9 + rg) * U + hd * 2048 + v16;
    lds_w128(kvs, kk); lds_w128(kvs + 1024, vv);
    qf = asfrag(join16(qp[0], qp[1]));
  }
  STAMP(3);
  if (w == 0) {
    int Tg = 2 * ti - 8 + ((l & 31) >> 2); Tg = Tg < 0 ? 2 * ti : Tg;
    wait_hint(rws, (unsigned)((Tg * 4 + (l & 3)) * 4), HN_K + (unsigned)cg * 1024, htag);
  }
  if (w == 7) {
#pragma unroll 1
    for (int spin = 0; spin < SPIN; ++spin) {
      v4u f0, f1; unsigned vo = l * 32, so = FL_0;
      asm volatile("buffer_load_dwordx4 %0, %2, %3, %4 offen sc1\n\tbuffer_load_dwordx4 %1, %2, %3, %4 offen offset:16 sc1\n\ts_waitcnt vmcnt(0)"
                   : "=&v"(f0), "=&v"(f1) : "v"(vo), "s"(rws), "s"(so) : "memory");
      bool ok = f0[0] == tag[0] && f0[1] == tag[1] && f0[2] == tag[0] && f0[3] == tag[1] && f1[0] == tag[0] && f1[1] == tag[1] && f1[2] == tag[0] && f1[3] == tag[1];
      if (__all(ok)) break;
      __builtin_amdgcn_s_sleep(4);
    }
    if (bid == 0) { unsigned long long e = epoch + 1ull; v2u en = {(unsigned)e, (unsigned)(e >> 32)}; unsigned z = 0, so = WS_EP; ST64_SC1(en, rws, z, so); }
  }
  const unsigned CONST = 11 * U;
  if (tid >= 64 && tid < 68) { unsigned v = (tid & 1) ? 0u : 0x3f803f80u; lds_w128(L + CONST + (tid & 1) * 16 + ((tid >> 1) & 1) * 512, (v4u){v, v, v, v}); }
  BAR_LDS();
  STAMP(4);
  { const int Tg = 2 * ti - 8 + w; if (Tg >= 0) fetch8k(rws, WS_KV + (unsigned)(Tg * 32 + cg * 4) * 2048, L, (1 + w) * U, L0, v16); }
  unsigned ovoff, dvoff;
  { int row = l >> 3, c = l & 7; ovoff = row * 1024 + ((c ^ (((row >> 1) & 3) << 1)) * 16);
    int swz = ((row >> 1) & 1) | ((w & 1) << 1); dvoff = row * 1024 + ((c ^ (swz << 1)) * 16); }
  BAR_LDS();
  STAMP(5);
#pragma unroll
  for (int b = 0; b < 8; b++) { int kbk = b * 8 + w; unsigned so = (unsigned)(kbk * 8192 + ch * 128); unsigned dst = L0 + 12 * U + kbk * 1024; DMA(rwo, dst, ovoff, so, 0); }
  { unsigned xv = (l >> 3) * 1024 + (l & 7) * 16; unsigned so = (unsigned)((tj * 64 + w * 8) * 1024 + ch * 128); unsigned dst = L0 + w * 1024; DMA(rx, dst, xv, so, 0); }
  v4u wq[32];
  const bool two = 8 + w < 2 * nks;
  const unsigned wt0 = WS_WT + (unsigned)(2 * ks0 + w) * 32768, wt1 = wt0 + 8 * 32768;
  auto wload = [&](int n, unsigned base) { unsigned so = base + (unsigned)((n & 1) * 16384 + (n >> 1) * 1024); GLOAD_SC1(wq[n], rws, v16, so); };
#pragma unroll
  for (int n = 0; n < P1; n++) wload(n, wt0);
  {
    const int qt = rg, r32 = l & 31, h = l >> 5;
    const int nskip = max(0, 8 - (2 * ti + qt));
    const ldsp kbase = L + (1 + qt) * U + hd * 2048 + v16;
    f32x16 zero;
#pragma unroll
    for (int r = 0; r < 16; r++) zero[r] = 0.f;
    const int qq = r32 - 4 * h;
    const unsigned vstride = (r32 < 16) ? U : 0u;
    const ldsp va = (r32 < 16) ? (L + (1 + qt) * U + hd * 2048 + 1024 + (h * 16 + r32) * 16) : (L + CONST + ((r32 == 16 || r32 == 20) ? 0u : 16u));
    f32x16 o = zero, sm;
    float m;
    if (nskip == 0) {
      bf16x8 kf[9];
#pragma unroll
      for (int kt = 0; kt < 8; kt++) kf[kt] = asfrag(lds_r128(kbase + kt * U));
      kf[8] = asfrag(lds_r128(kbase + 4 * U + 4 * U));
      SB();
      {
        f32x16 s8 = MFMA32(kf[8], qf, zero), s0 = MFMA32(kf[0], qf, zero);
        f32x16 sa = MFMA32(kf[1], qf, zero), sb = MFMA32(kf[2], qf, zero);
        SB();
#pragma unroll
        for (int r = 0; r < 16; r++) { const int kc = (r & 3) + 8 * (r >> 2); sm[r] = (qq >= kc) ? s8[r] : s0[r]; }
        s8 = MFMA32(kf[3], qf, zero); s0 = MFMA32(kf[4], qf, zero);
        float m1 = tmax(sm, -3.0e38f), m2 = tmax(sa, -3.0e38f);
        sa = MFMA32(kf[5], qf, zero);
        m1 = tmax(sb, m1);
        sb = MFMA32(kf[6], qf, zero);
        m2 = tmax(s8, m2);
        s8 = MFMA32(kf[7], qf, zero);
        m1 = tmax(s0, m1); m2 = tmax(sa, m2); m1 = tmax(sb, m1); m2 = tmax(s8, m2);
        m = __builtin_fmaxf(m1, m2);
      }
      { auto rr = __builtin_amdgcn_permlane32_swap(__float_as_uint(m), __float_as_uint(m), false, false); m = __builtin_fmaxf(__uint_as_float(rr[0]), __uint_as_float(rr[1])); }
      f32x16 negm;
#pragma unroll
      for (int r = 0; r < 16; r++) negm[r] = -m;
      f32x16 sc = MFMA32(kf[1], qf, negm), sn;
      v4u vf[2][2];
      {
        ldsp v8 = va + 8 * vstride;
        v4u v80 = lds_r128(v8), v81 = lds_r128(v8 + 512), v00 = lds_r128(va), v01 = lds_r128(va + 512);
        vf[1][0] = lds_r128(va + vstride); vf[1][1] = lds_r128(va + vstride + 512);
#pragma unroll
        for (int r = 0; r < 16; r++) sm[r] -= m;
        v4u p0, p1; pexp(sm, p0, p1);
        v4u a0, a1, b0, b1;
#pragma unroll
        for (int j = 0; j < 4; j++) {
          const int kc0 = ((2 * j) & 3) + 8 * ((2 * j) >> 2), kc1 = kc0 + 16;
          unsigned m0 = (qq > kc0) ? 0xffffffffu : ((qq == kc0) ? 0xffffu : 0u);
          unsigned m1 = (qq > kc1) ? 0xffffffffu : ((qq == kc1) ? 0xffffu : 0u);
          a0[j] = p0[j] & m0; b0[j] = p0[j] ^ a0[j]; a1[j] = p1[j] & m1; b1[j] = p1[j] ^ a1[j];
        }
        o = MFMA32(asfrag(v80), asfrag(a0), o); o = MFMA32(asfrag(v81), asfrag(a1), o);
        o = MFMA32(asfrag(v00), asfrag(b0), o); o = MFMA32(asfrag(v01), asfrag(b1), o);
      }
#pragma unroll
      for (int kt = 1; kt < 8; kt++) {
        if (kt < 7) {
          sn = MFMA32(kf[kt + 1], qf, negm);
          ldsp vn = va + (kt + 1) * vstride;
          vf[(kt + 1) & 1][0] = lds_r128(vn); vf[(kt + 1) & 1][1] = lds_r128(vn + 512);
        }
        SB();
        v4u p0, p1; pexp(sc, p0, p1);
        o = MFMA32(asfrag(vf[kt & 1][0]), asfrag(p0), o); o = MFMA32(asfrag(vf[kt & 1][1]), asfrag(p1), o);
        SB();
        sc = sn;
      }
    } else {
      {
        f32x16 s8 = MFMA32(asfrag(lds_r128(kbase + 4 * U + 4 * U)), qf, zero);
#pragma unroll
        for (int r = 0; r < 16; r++) { const int kc = (r & 3) + 8 * (r >> 2); sm[r] = (qq >= kc) ? s8[r] : -3.0e38f; }
        m = tmax(sm, -3.0e38f);
      }
#pragma unroll 1
      for (int kt = nskip; kt < 8; kt++) { f32x16 s = MFMA32(asfrag(lds_r128(kbase + kt * U)), qf, zero); m = tmax(s, m); }
      { auto rr = __builtin_amdgcn_permlane32_swap(__float_as_uint(m), __float_as_uint(m), false, false); m = __builtin_fmaxf(__uint_as_float(rr[0]), __uint_as_float(rr[1])); }
      f32x16 negm;
#pragma unroll
      for (int r = 0; r < 16; r++) { negm[r] = -m; sm[r] -= m; }
      {
        v4u p0, p1; pexp(sm, p0, p1);
        ldsp v8 = va + 8 * vstride;
        o = MFMA32(asfrag(lds_r128(v8)), asfrag(p0), o); o = MFMA32(asfrag(lds_r128(v8 + 512)), asfrag(p1), o);
      }
#pragma unroll 1
      for (int kt = nskip; kt < 8; kt++) {
        f32x16 s = MFMA32(asfrag(lds_r128(kbase + kt * U)), qf, negm);
        v4u p0, p1; pexp(s, p0, p1);
        ldsp vn = va + kt * vstride;
        o = MFMA32(asfrag(lds_r128(vn)), asfrag(p0), o); o = MFMA32(asfrag(lds_r128(vn + 512)), asfrag(p1), o);
      }
    }
    float inv = __builtin_amdgcn_rcpf(o[8]);
    v4u ov;
#pragma unroll
    for (int j = 0; j < 4; j++) ov[j] = pks(o[2 * j] * inv, o[2 * j + 1] * inv);
    STAMP(6);
    GSTORE_SC1(ov, rws, vo_o, so_o);
    { unsigned z = 0, sh = HN_O + (unsigned)(ti * 64 + cg * 8 + w) * 4; ST32_SC1(htag, rws, z, sh); }
  }
  BAR_VM(2);
  STAMP(7);
  auto dunit = [&](int j) -> unsigned { return j < 8 ? 3 + j : (j < 10 ? j - 7 : (j == 10 ? 0 : (j < 19 ? j + 1 : j - 16))); };
  auto dissue = [&](int j) {
    unsigned dst = L0 + dunit(j) * U + w * 1024, so = (unsigned)((j * 8 + w) * 8192 + ch * 128);
    DMA(rwd, dst, dvoff, so, 0);
  };
  auto dissue_np = [&](int j0, int j1) {
    if (w != 0) {
#pragma unroll
      for (int j = j0; j < j1; j++) dissue(j);
    }
    if (w == 2) {
#pragma unroll
      for (int j = j0; j < j1; j++) { unsigned dst = L0 + dunit(j) * U, so = (unsigned)(j * 8 * 8192 + ch * 128); DMA(rwd, dst, dvoff, so, 0); }
    }
  };
  const int rt = w >> 1, kh = w & 1;
  v4u hv;
  {
    dissue_np(0, 8);
    const int row8 = 4 * (g & 1) + q4, swz8 = (row8 >> 1) & 3;
    bf16x8 wf[4][4];
    ldsp tb[4];
#pragma unroll
    for (int n = 0; n < 4; n++) tb[n] = L + 12 * U + kh * 32768 + (g >> 1) * 2048 + row8 * 128 + p4 * 8 + ((n ^ swz8) * 32);
    if (w == 0) wait_hint(rws, (unsigned)(l * 4), HN_O + (unsigned)tj * 256, htag);
    asm volatile("s_barrier" ::: "memory");
    STAMP(8);
    f32x4 acc[4];
#pragma unroll 1
    for (int spin = 0; spin < SPIN; ++spin) {
      v4u of[8];
#pragma unroll
      for (int k = 0; k < 8; k++) { unsigned so = WS_O + (unsigned)((tj * 4 + rt) * 16 + kh * 8 + k) * 1024; GLOAD_SC1(of[k], rws, v16, so); }
#pragma unroll
      for (int k = 0; k < 4; k++)
#pragma unroll
        for (int n = 0; n < 4; n++) wf[k][n] = trfrag(tb[n] + k * 4096, tb[n] + k * 4096 + 1024);
#pragma unroll
      for (int n = 0; n < 4; n++) acc[n] = (f32x4){0, 0, 0, 0};
      unsigned pm = 0;
      SB();
#define WO_STEP(k, N) asm volatile("s_waitcnt vmcnt(" #N ")" : "+v"(of[k])::"memory"); SB(); \
      _Pragma("unroll") for (int n = 0; n < 4; n++) acc[n] = MFMA16(wf[(k) & 3][n], asfrag(of[k]), acc[n]); pm = pchk(of[k], pm); SB(); \
      if ((k) < 4) { _Pragma("unroll") for (int n = 0; n < 4; n++) wf[(k) & 3][n] = trfrag(tb[n] + ((k) + 4) * 4096, tb[n] + ((k) + 4) * 4096 + 1024); } SB();
      WO_STEP(0, 7) WO_STEP(1, 6) WO_STEP(2, 5) WO_STEP(3, 4) WO_STEP(4, 3) WO_STEP(5, 2) WO_STEP(6, 1) WO_STEP(7, 0)
      if (!__any(pm == 0xffffffffu)) break;
    }
    {
      f32x4 s0 = kh ? acc[0] : acc[2], s1 = kh ? acc[1] : acc[3];
      lds_w128(L + 1 * U + w * 2048 + v16, __builtin_bit_cast(v4u, s0));
      lds_w128(L + 1 * U + w * 2048 + 1024 + v16, __builtin_bit_cast(v4u, s1));
    }
    const ldsp xa = L + (rt * 16 + c16) * 128 + kh * 64 + g * 8;
    v2u x0 = lds_r64(xa), x1 = lds_r64(xa + 32);
    BAR_LDS();
    {
      f32x4 r0 = asf(lds_r128(L + 1 * U + (w ^ 1) * 2048 + v16)), r1 = asf(lds_r128(L + 1 * U + (w ^ 1) * 2048 + 1024 + v16));
      f32x4 a0 = (kh ? acc[2] : acc[0]) + r0, a1 = (kh ? acc[3] : acc[1]) + r1;
      hv[0] = pks(lo16(x0[0]) + bfr(a0[0]), hi16(x0[0]) + bfr(a0[1])); hv[1] = pks(lo16(x0[1]) + bfr(a0[2]), hi16(x0[1]) + bfr(a0[3]));
      hv[2] = pks(lo16(x1[0]) + bfr(a1[0]), hi16(x1[0]) + bfr(a1[1])); hv[3] = pks(lo16(x1[1]) + bfr(a1[2]), hi16(x1[1]) + bfr(a1[3]));
      STAMP(9);
      GSTORE_SC1(hv, rws, v16, so_h);
      { unsigned z = 0, sh = HN_H + (unsigned)(tj * 64 + ch * 8 + w) * 4; ST32_SC1(htag, rws, z, sh); }
    }
  }
  v4u cp[4];
  {
    const unsigned cvo = (unsigned)(c16 * 64 + g * 16);
#pragma unroll
    for (int t = 0; t < 4; t++) {
      int tile = (2 * ks0 + w + ((t >> 1) && two ? 8 : 0)) * 2 + (t & 1);
      unsigned so = WS_CP + (unsigned)tile * 1024; GLOAD_SC1(cp[t], rws, cvo, so);
    }
    if (w != 0) {
#pragma unroll
      for (int n = P1; n < 32; n++) wload(n, wt0);
    }
    if (w == 0) wait_hint(rws, (unsigned)(l * 4), HN_H + (unsigned)tj * 256, htag);
    BAR_LDS();
    STAMP(10);
    fetch8k(rws, WS_H + (unsigned)((tj * 4 + rt) * 16 + kh * 8) * 1024, L, (12 + w) * U, L0, v16);
    STAMP(11);
    asm volatile("s_barrier" : "+v"(cp[0]), "+v"(cp[1]), "+v"(cp[2]), "+v"(cp[3])::"memory");
#pragma unroll
    for (int n = 0; n < P1; n += 4) asm volatile("" : "+v"(wq[n]), "+v"(wq[n + 1]), "+v"(wq[n + 2]), "+v"(wq[n + 3]));
    if (w == 0) {
#pragma unroll
      for (int n = P1; n < 32; n++) wload(n, wt0);
    }
    dissue(8); dissue(9); dissue(10);
  }
  f32x4 c4[4];
  STAMP(12);
  {
    const ldsp ab = L + 12 * U + v16;
    const ldsp abs_ = ab + (w >> 1) * 16384 + (w & 1) * 1024;
    const ldsp TB = L + 11 * U;
    float ri[4], mr[4];
    const unsigned avo = v16;
    auto pass = [&](auto PASS) {
      constexpr int ps = decltype(PASS)::value;
      const unsigned ht_ = htag, av_ = avo; const v4u rw_ = rws;
      f32x4 ag[4], au[4];
#pragma unroll
      for (int r = 0; r < 4; r++) { ag[r] = (f32x4){0, 0, 0, 0}; au[r] = (f32x4){0, 0, 0, 0}; }
      float s0 = 0.f, s1 = 0.f, q0 = 0.f, q1 = 0.f;
      bf16x8 ha[2][4];
      v4u hs[2];
      auto rd = [&](int ks) {
#pragma unroll
        for (int r = 0; r < 4; r++) ha[ks & 1][r] = asfrag(lds_r128(ab + (r * 16 + ks) * 1024));
        if (ps == 0 && (ks % 3) != 2 && ks < 12) hs[(ks - ks / 3) & 1] = lds_r128(abs_ + (ks - ks / 3) * 2048);
      };
      rd(0);
#pragma unroll
      for (int ks = 0; ks < 16; ks++) {
        const int b = ks & 1;
        const bool st = ps == 0 && (ks % 3) != 2 && ks < 12;
        const int sf = (ks - ks / 3) & 1;
        if (ps == 1) {
          switch (ks) {
#define WV(K, N) case K: asm volatile("s_waitcnt vmcnt(" #N ")" : "+v"(wq[2 * K]), "+v"(wq[2 * K + 1])::"memory"); break;
            WV(0, 30) WV(1, 28) WV(2, 26) WV(3, 24) WV(4, 22) WV(5, 20) WV(6, 18) WV(7, 16) WV(8, 14) WV(9, 12) WV(10, 10) WV(11, 8) WV(12, 6) WV(13, 4) WV(14, 2) WV(15, 0)
          }
        }
        if (ps == 0 && ks == 12) asm volatile("s_waitcnt vmcnt(27)" : "+v"(wq[24]), "+v"(wq[25]), "+v"(wq[26]), "+v"(wq[27]), "+v"(wq[28]), "+v"(wq[29]), "+v"(wq[30]), "+v"(wq[31])::"memory");
        if (ps == 0 && ks == 13) BAR_LDS();
        SB();
        ag[0] = MFMA16(asfrag(wq[2 * ks]), ha[b][0], ag[0]);
        SB();
        if (ks < 15) rd(ks + 1);
        SB();
        ag[1] = MFMA16(asfrag(wq[2 * ks]), ha[b][1], ag[1]);
        SB(); if (st) STAT(hs[sf][0], s0, s1, q0, q1); SB();
        ag[2] = MFMA16(asfrag(wq[2 * ks]), ha[b][2], ag[2]);
        ag[3] = MFMA16(asfrag(wq[2 * ks]), ha[b][3], ag[3]);
        SB(); if (st) STAT(hs[sf][1], s0, s1, q0, q1); SB();
        au[0] = MFMA16(asfrag(wq[2 * ks + 1]), ha[b][0], au[0]);
        au[1] = MFMA16(asfrag(wq[2 * ks + 1]), ha[b][1], au[1]);
        SB(); if (st) STAT(hs[sf][2], s0, s1, q0, q1); SB();
        au[2] = MFMA16(asfrag(wq[2 * ks + 1]), ha[b][2], au[2]);
        au[3] = MFMA16(asfrag(wq[2 * ks + 1]), ha[b][3], au[3]);
        SB(); if (st) STAT(hs[sf][3], s0, s1, q0, q1); SB();
        if (ps == 0 && two) { wload(2 * ks, wt1); wload(2 * ks + 1, wt1); }
        if (ps == 0 && ks == 12) {
          float ss = sum_g(s0 + s1), qq = sum_g(q0 + q1);
          if (g == 0) lds_w64(TB + 1024 + (w & 1) * 512 + ((w >> 1) * 16 + c16) * 8, (v2u){__float_as_uint(ss), __float_as_uint(qq)});
        }
        if (ps == 0 && ks == 13) {
          v2u a = lds_r64(TB + 1024 + l * 8), b2 = lds_r64(TB + 1024 + 512 + l * 8);
          float rinv, m_;
          ln_scalars(__uint_as_float(a[0]) + __uint_as_float(b2[0]), __uint_as_float(a[1]) + __uint_as_float(b2[1]), rinv, m_);
          lds_w64(TB + 4096 + w * 512 + l * 8, (v2u){__float_as_uint(rinv), __float_as_uint(m_)});
        }
        if (ps == 0 && ks == 14) {
#pragma unroll
          for (int r = 0; r < 4; r++) { v2u s = lds_r64(TB + 4096 + w * 512 + (r * 16 + c16) * 8); ri[r] = __uint_as_float(s[0]); mr[r] = __uint_as_float(s[1]); }
        }
        if (ps == 0 && (ks == 2 || ks == 5 || ks == 8 || ks == 11)) c4[ks / 3] = sum16x4(cp[ks / 3]);
        SB();
      }
      v2u ap[4];
#pragma unroll
      for (int r = 0; r < 4; r++) {
        f32x4 gg = (ag[r] * ri[r] - c4[2 * ps] * mr[r]) * -1.4426950408889634f, uu = au[r] * ri[r] - c4[2 * ps + 1] * mr[r];
        float a[4];
#pragma unroll
        for (int j = 0; j < 4; j++) a[j] = uu[j] * __builtin_amdgcn_rcpf(1.f + __builtin_amdgcn_exp2f(gg[j]));
        ap[r] = (v2u){pks(a[0], a[1]), pks(a[2], a[3])};
      }
      const int pg = 2 * ks0 + w + 8 * ps;
      v4u b0 = join16(ap[0], ap[1]), b1 = join16(ap[2], ap[3]);
      unsigned so = WS_ACT + (unsigned)(tj * 2 * 86 + pg) * 1024, s2 = so + 86 * 1024;
      GSTORE_SC1(b0, rw_, av_, so); GSTORE_SC1(b1, rw_, av_, s2);
      { unsigned z = 0, sh = HN_A + (unsigned)(tj * 128 + pg) * 4; ST32_SC1(ht_, rw_, z, sh); }
    };
    pass(std::integral_constant<int, 0>{});
    STAMP(13);
    if (two) pass(std::integral_constant<int, 1>{});
  }
  STAMP(14);
  {
    constexpr int NPRE = 7;
    int l5 = threadIdx.x & 63; asm volatile("" : "+v"(l5));
    const int l = l5, c16 = l & 15, g = l >> 4, q4 = (l >> 2) & 3, p4 = l & 3;
    const unsigned v16 = l * 16;
    const int swq = ((q4 >> 1) & 1) | ((g & 1) << 1);
    ldsp tb[4], tc[4], td[4];
#pragma unroll
    for (int n = 0; n < 4; n++) { tb[n] = L + kh * 4096 + g * 1024 + q4 * 128 + p4 * 8 + ((n ^ swq) * 32); tc[n] = tb[n] + 7 * U; td[n] = tb[n] + 14 * U; }
    bf16x8 wf[NPRE][4];
    auto rd = [&](int j) {
      const unsigned u = dunit(j);
#pragma unroll
      for (int n = 0; n < 4; n++) { const ldsp b = u < 7 ? tb[n] + u * U : (u < 14 ? tc[n] + (u - 7) * U : td[n] + (u - 14) * U); wf[j % NPRE][n] = trfrag(b, b + 512); }
    };
    BAR_VM(3);
    dissue_np(11, 19);
#pragma unroll
    for (int j = 0; j < NPRE; j++) rd(j);
    if (w == 0) {
      unsigned v0 = (unsigned)(l * 4), v1 = (unsigned)((l < 22 ? 64 + l : l) * 4);
      wait_hint(rws, v0, HN_A + (unsigned)tj * 512, htag);
      wait_hint(rws, v1, HN_A + (unsigned)tj * 512, htag);
    }
    BAR_LDS();
    STAMP(15);
    const unsigned avo = (unsigned)((g >> 1) * 1024 + (g & 1) * 512 + ((rt & 1) * 16 + c16) * 16);
    const unsigned aso = WS_ACT + (unsigned)((tj * 2 + (rt >> 1)) * 86 + 2 * kh) * 1024;
    const unsigned aso21 = aso + (unsigned)(kh ? 20 : 21) * 4096;
    v4u af[22];
#pragma unroll
    for (int j = 0; j < 22; j++) { unsigned so = j == 21 ? aso21 : aso + (unsigned)j * 4096; GLOAD_SC1(af[j], rws, avo, so); }
    dissue(19); dissue(20); dissue(21);
    f32x4 acc[4];
#pragma unroll
    for (int n = 0; n < 4; n++) acc[n] = (f32x4){0, 0, 0, 0};
#define DN_STEP(j, N) \
    asm volatile("s_waitcnt vmcnt(" #N ")" : "+v"(af[j])::"memory"); \
    if (__builtin_expect(__any(pchk(af[j], 0u) == 0xffffffffu), 0)) { \
      _Pragma("unroll 1") for (int spin = 0; spin < SPIN; ++spin) { \
        unsigned so = (j) == 21 ? aso21 : aso + (unsigned)(j) * 4096; \
        asm volatile("buffer_load_dwordx4 %0, %1, %2, %3 offen sc1\n\ts_waitcnt vmcnt(0)" : "=&v"(af[j]) : "v"(avo), "s"(rws), "s"(so) : "memory"); \
        if (!__any(pchk(af[j], 0u) == 0xffffffffu)) break; \
      } \
    } \
    SB(); \
    if ((j) < 21 || kh == 0) { _Pragma("unroll") for (int n = 0; n < 4; n++) acc[n] = MFMA16(wf[(j) % NPRE][n], asfrag(af[j]), acc[n]); } \
    SB(); \
    if ((j) + NPRE < 22) rd((j) + NPRE); \
    SB();
    DN_STEP(0, 24) DN_STEP(1, 23) DN_STEP(2, 22) DN_STEP(3, 21)
    asm volatile("s_barrier" ::: "memory");
    DN_STEP(4, 20) DN_STEP(5, 19) DN_STEP(6, 18) DN_STEP(7, 17) DN_STEP(8, 16) DN_STEP(9, 15) DN_STEP(10, 14) DN_STEP(11, 13)
    asm volatile("s_waitcnt vmcnt(0)\n\ts_barrier" : "+v"(af[12]), "+v"(af[13]), "+v"(af[14]), "+v"(af[15]), "+v"(af[16]), "+v"(af[17]), "+v"(af[18]), "+v"(af[19]),
                 "+v"(af[20]), "+v"(af[21])::"memory");
    DN_STEP(12, 0) DN_STEP(13, 0) DN_STEP(14, 0) DN_STEP(15, 0) DN_STEP(16, 0) DN_STEP(17, 0) DN_STEP(18, 0) DN_STEP(19, 0) DN_STEP(20, 0) DN_STEP(21, 0)
    STAMP(16);
    {
      f32x4 s0 = kh ? acc[0] : acc[2], s1 = kh ? acc[1] : acc[3];
      lds_w128(L + 1 * U + w * 2048 + v16, __builtin_bit_cast(v4u, s0));
      lds_w128(L + 1 * U + w * 2048 + 1024 + v16, __builtin_bit_cast(v4u, s1));
    }
    BAR_LDS();
    {
      f32x4 r0 = asf(lds_r128(L + 1 * U + (w ^ 1) * 2048 + v16)), r1 = asf(lds_r128(L + 1 * U + (w ^ 1) * 2048 + 1024 + v16));
      f32x4 a0 = (kh ? acc[2] : acc[0]) + r0, a1 = (kh ? acc[3] : acc[1]) + r1;
      v2u o0 = {pk(lo16(hv[0]) + bfr(a0[0]), hi16(hv[0]) + bfr(a0[1])), pk(lo16(hv[1]) + bfr(a0[2]), hi16(hv[1]) + bfr(a0[3]))};
      v2u o1 = {pk(lo16(hv[2]) + bfr(a1[0]), hi16(hv[2]) + bfr(a1[1])), pk(lo16(hv[3]) + bfr(a1[2]), hi16(hv[3]) + bfr(a1[3]))};
      v4u ov = join16(o0, o1);
      unsigned vo = (unsigned)(c16 * 1024 + (g & 1) * 32 + (g >> 1) * 16), so = (unsigned)((tj * 64 + rt * 16) * 1024 + ch * 128 + kh * 64);
      GSTORE(ov, rout, vo, so);
    }
  }
}

extern "C" void kernel_launch(void* const* d_in, const int* in_sizes, int n_in, void* d_out, int out_size, void* d_ws, size_t ws_size,
                              hipStream_t stream) {
  (void)hipFuncSetAttribute((const void*)fused_block, hipFuncAttributeMaxDynamicSharedMemorySize, LDS_BYTES);
  fused_block<<<256, 512, LDS_BYTES, stream>>>(d_in[0], d_in[1], d_in[2], d_in[4], d_in[5], d_in[6], d_ws, d_out);
}
```

```cpp
#include <hip/hip_runtime.h>
#include <hip/hip_bf16.h>
#include <type_traits>

typedef unsigned v4u __attribute__((ext_vector_type(4)));
typedef unsigned v2u __attribute__((ext_vector_type(2)));
typedef float f32x2 __attribute__((ext_vector_type(2)));
typedef float f32x4 __attribute__((ext_vector_type(4)));
typedef float f32x16 __attribute__((ext_vector_type(16)));
typedef short bf16x8 __attribute__((ext_vector_type(8)));
typedef short s16x4 __attribute__((ext_vector_type(4)));
typedef __bf16 bf16x2_t __attribute__((ext_vector_type(2)));
#define LAS __attribute__((address_space(3)))
#define DI __device__ __forceinline__

constexpr int S_ = 2048, D_ = 512, F_ = 1376;
constexpr unsigned WS_KV = 0;
constexpr unsigned WS_WD = 4u << 20;
constexpr unsigned WS_O = WS_KV + (6u << 20);
constexpr unsigned WS_H = WS_O + (2u << 20);
constexpr unsigned WS_ACT = WS_H + (2u << 20);
constexpr unsigned WS_WT = 16u << 20;
constexpr unsigned WS_CP = 19u << 20;
constexpr unsigned WS_SP = WS_CP + (512u << 10);
constexpr unsigned WS_FL = 20u << 20;
constexpr unsigned FL_K = WS_FL;
constexpr unsigned FL_O = FL_K + 256 * 128;
constexpr unsigned FL_H = FL_O + 512 * 128;
constexpr unsigned FL_A = FL_H + 512 * 128;
constexpr unsigned WS_EP = FL_A + 1024 * 128;
constexpr unsigned WS_END = WS_EP + 256;
constexpr int LDS_BYTES = 160 * 1024;
constexpr unsigned U = 8192;
constexpr unsigned OOB = 0x7ffffff0u;
constexpr int SPIN = 40000;

DI v4u mk_rsrc(const void* p, unsigned bytes) {
  unsigned long long a = (unsigned long long)p;
  v4u r; r.x = (unsigned)a; r.y = (unsigned)(a >> 32) & 0xffffu; r.z = bytes; r.w = 0x00020000u; return r;
}
DI unsigned pk(float a, float b) { f32x2 v = {a, b}; bf16x2_t r = __builtin_convertvector(v, bf16x2_t); return __builtin_bit_cast(unsigned, r); }
DI float bfr(float a) { return __uint_as_float(pk(a, a) << 16); }
DI float lo16(unsigned u) { return __uint_as_float(u << 16); }
DI float hi16(unsigned u) { return __uint_as_float(u & 0xffff0000u); }
DI float dot_one(unsigned u, float c) { return __builtin_amdgcn_fdot2_f32_bf16(__builtin_bit_cast(bf16x2_t, u), __builtin_bit_cast(bf16x2_t, 0x3f803f80u), c, false); }
DI float dot_sq(unsigned u, float c) { return __builtin_amdgcn_fdot2_f32_bf16(__builtin_bit_cast(bf16x2_t, u), __builtin_bit_cast(bf16x2_t, u), c, false); }

#define DMA(rsrc, lds_addr, voff, soff, OFF) \
  asm volatile("s_mov_b32 m0, %0\n\ts_nop 0\n\tbuffer_load_dwordx4 %1, %2, %3 offen offset:" #OFF " lds" ::"s"(lds_addr), "v"(voff), "s"(rsrc), "s"(soff) : "memory")
#define DMA_SC1(rsrc, lds_addr, voff, soff, OFF) \
  asm volatile("s_mov_b32 m0, %0\n\ts_nop 0\n\tbuffer_load_dwordx4 %1, %2, %3 offen offset:" #OFF " sc1 lds" ::"s"(lds_addr), "v"(voff), "s"(rsrc), "s"(soff) : "memory")
#define GLOAD_SC1(dst, rsrc, voff, soff) \
  asm volatile("buffer_load_dwordx4 %0, %1, %2, %3 offen sc1" : "=v"(dst) : "v"(voff), "s"(rsrc), "s"(soff) : "memory")
#define GSTORE(val, rsrc, voff, soff) \
  asm volatile("buffer_store_dwordx4 %0, %1, %2, %3 offen\n\ts_nop 1" ::"v"(val), "v"(voff), "s"(rsrc), "s"(soff) : "memory")
#define GSTORE_SC1(val, rsrc, voff, soff) \
  asm volatile("buffer_store_dwordx4 %0, %1, %2, %3 offen sc1\n\ts_nop 1" ::"v"(val), "v"(voff), "s"(rsrc), "s"(soff) : "memory")
#define ST64_SC1(val, rsrc, voff, soff) \
  asm volatile("buffer_store_dwordx2 %0, %1, %2, %3 offen sc1\n\ts_nop 1" ::"v"(val), "v"(voff), "s"(rsrc), "s"(soff) : "memory")
#define ST32_SC1(val, rsrc, voff, soff) \
  asm volatile("buffer_store_dword %0, %1, %2, %3 offen sc1" ::"v"(val), "v"(voff), "s"(rsrc), "s"(soff) : "memory")
#define WAIT_VM_(N) asm volatile("s_waitcnt vmcnt(" #N ")" ::: "memory")
#define WAIT_VM(N) WAIT_VM_(N)
#define BAR_VM(N) asm volatile("s_waitcnt vmcnt(" #N ") lgkmcnt(0)\n\ts_barrier" ::: "memory")
#define BAR_LDS() asm volatile("s_waitcnt lgkmcnt(0)\n\ts_barrier" ::: "memory")
#define SB() __builtin_amdgcn_sched_barrier(0)
#define MFMA16(a, b, c) __builtin_amdgcn_mfma_f32_16x16x32_bf16((a), (b), (c), 0, 0, 0)
#define MFMA32(a, b, c) __builtin_amdgcn_mfma_f32_32x32x16_bf16((a), (b), (c), 0, 0, 0)

typedef LAS char* ldsp;
DI s16x4 trd(ldsp a) { return __builtin_amdgcn_ds_read_tr16_b64_v4i16((LAS s16x4*)a); }
DI bf16x8 trfrag(ldsp a0, ldsp a1) { s16x4 lo = trd(a0), hi = trd(a1); return __builtin_shufflevector(lo, hi, 0, 1, 2, 3, 4, 5, 6, 7); }
DI v4u lds_r128(ldsp a) { return *(LAS v4u*)a; }
DI void lds_w128(ldsp a, v4u v) { *(LAS v4u*)a = v; }
DI void lds_w64(ldsp a, v2u v) { *(LAS v2u*)a = v; }
DI v2u lds_r64(ldsp a) { return *(LAS v2u*)a; }
DI bf16x8 asfrag(v4u v) { return __builtin_bit_cast(bf16x8, v); }
DI v4u asu(bf16x8 v) { return __builtin_bit_cast(v4u, v); }
DI f32x4 asf(v4u v) { return __builtin_bit_cast(f32x4, v); }

template <int CTRL> DI float dpp_add(float v) {
  int t = __builtin_amdgcn_update_dpp(0, __float_as_int(v), CTRL, 0xf, 0xf, false);
  return v + __int_as_float(t);
}
DI float swap32_add(float a, float b) { auto r = __builtin_amdgcn_permlane32_swap(__float_as_uint(a), __float_as_uint(b), false, false); return __uint_as_float(r[0]) + __uint_as_float(r[1]); }
DI float swap16_add(float a, float b) { auto r = __builtin_amdgcn_permlane16_swap(__float_as_uint(a), __float_as_uint(b), false, false); return __uint_as_float(r[0]) + __uint_as_float(r[1]); }
DI float sum_g(float v) { v = swap16_add(v, v); return swap32_add(v, v); }
DI float sum8(float v) { v = dpp_add<0xB1>(v); v = dpp_add<0x4E>(v); return dpp_add<0x141>(v); }
DI v4u join16(v2u a, v2u b) {
  auto r0 = __builtin_amdgcn_permlane16_swap(a[0], b[0], false, false), r1 = __builtin_amdgcn_permlane16_swap(a[1], b[1], false, false);
  return (v4u){r0[0], r1[0], r0[1], r1[1]};
}
DI v4u join32(v2u a, v2u b) {
  auto r0 = __builtin_amdgcn_permlane32_swap(a[0], b[0], false, false), r1 = __builtin_amdgcn_permlane32_swap(a[1], b[1], false, false);
  return (v4u){r0[0], r1[0], r0[1], r1[1]};
}
DI void ln_scalars(float s, float q, float& rinv, float& mr) {
  float mean = s * (1.f / 512.f);
  float var = __builtin_fmaxf(__builtin_fmaf(-mean, mean, q * (1.f / 512.f)), 0.f);
  float mb = bfr(mean), vb = bfr(var), t = bfr(vb + 1e-5f), sd = bfr(__builtin_amdgcn_sqrtf(t));
  rinv = __builtin_amdgcn_rcpf(sd); mr = mb * rinv;
}
DI f32x2 dup2(float v) { f32x2 r = {v, v}; asm volatile("" : "+v"(r)); return r; }
DI f32x4 axmby(f32x4 a, f32x2 x, f32x4 b, f32x2 y) {
  f32x2 lo = (f32x2){a[0], a[1]} * x - (f32x2){b[0], b[1]} * y, hi = (f32x2){a[2], a[3]} * x - (f32x2){b[2], b[3]} * y;
  return (f32x4){lo[0], lo[1], hi[0], hi[1]};
}
DI float mx3(float a, float b, float c) { return __builtin_fmaxf(__builtin_fmaxf(a, b), c); }
DI float tmax(const f32x16& s, float m) {
#pragma unroll
  for (int r = 0; r < 16; r += 2) m = mx3(m, s[r], s[r + 1]);
  return m;
}
DI void pexp(const f32x16& s, v4u& p0, v4u& p1) {
#pragma unroll
  for (int j = 0; j < 4; j++) {
    p0[j] = pk(__builtin_amdgcn_exp2f(s[2 * j]), __builtin_amdgcn_exp2f(s[2 * j + 1]));
    p1[j] = pk(__builtin_amdgcn_exp2f(s[8 + 2 * j]), __builtin_amdgcn_exp2f(s[8 + 2 * j + 1]));
  }
}


DI void wait_flags(v4u rws, unsigned voff, unsigned soff, v2u tag) {
#pragma unroll 1
  for (int spin = 0; spin < SPIN; ++spin) {
    v2u f;
    asm volatile("buffer_load_dwordx2 %0, %1, %2, %3 offen sc1\n\ts_waitcnt vmcnt(0)" : "=&v"(f) : "v"(voff), "s"(rws), "s"(soff) : "memory");
    if (__all(f[0] == tag[0] && f[1] == tag[1])) break;
    __builtin_amdgcn_s_sleep(2);
  }
}
DI unsigned lds_inc(unsigned addr) {
  unsigned r, one = 1u;
  asm volatile("s_waitcnt lgkmcnt(0)\n\ts_mov_b64 exec, 1\n\tds_add_rtn_u32 %0, %1, %2\n\ts_mov_b64 exec, -1\n\ts_waitcnt lgkmcnt(0)" : "=&v"(r) : "v"(addr), "v"(one) : "memory");
  return (unsigned)__builtin_amdgcn_readfirstlane(r);
}
DI void lds_wait(unsigned addr, unsigned n) {
#pragma unroll 1
  for (int spin = 0; spin < (SPIN << 4); ++spin) {
    unsigned r;
    asm volatile("ds_read_b32 %0, %1\n\ts_waitcnt lgkmcnt(0)" : "=&v"(r) : "v"(addr) : "memory");
    if ((unsigned)__builtin_amdgcn_readfirstlane(r) >= n) break;
    __builtin_amdgcn_s_sleep(1);
  }
}
DI int early_rt(int e, int rot) { int c = ((e >> 2) + rot) & 7; return (c < 3 ? 6 * c : 5 * c + 3) + (e & 3); }

__global__ __launch_bounds__(512, 2) __attribute__((amdgpu_waves_per_eu(2, 2)))
void fused_block(const void* x, const void* wqkv, const void* wo, const void* wg, const void* wu, const void* wd, void* ws, void* out) {
  extern __shared__ __attribute__((aligned(16))) char smem[];
  const ldsp L = (ldsp)smem;
  const unsigned L0 = (unsigned)(size_t)L;
  const int bid = blockIdx.x;
  const int ti = ((bid & 7) >> 1) * 8 + (bid >> 5), cg = (bid & 1) * 4 + ((bid >> 3) & 3);
  const int tj = ti, ch = cg;
  const int tid = threadIdx.x, l = tid & 63, w = __builtin_amdgcn_readfirstlane(tid >> 6);
  const int c16 = l & 15, g = l >> 4, q4 = (l >> 2) & 3, p4 = l & 3;
  const v4u rx = mk_rsrc(x, S_ * D_ * 2), rwq = mk_rsrc(wqkv, D_ * 1536 * 2), rwo = mk_rsrc(wo, D_ * D_ * 2);
  const v4u rwg = mk_rsrc(wg, D_ * F_ * 2), rwu = mk_rsrc(wu, D_ * F_ * 2), rwd = mk_rsrc(wd, F_ * D_ * 2);
  const v4u rws = mk_rsrc(ws, WS_END), rout = mk_rsrc(out, S_ * D_ * 2);
  const unsigned v16 = l * 16;
  const int team = w >> 2, hd = w & 3;
  const int nks = ch < 3 ? 6 : 5, ks0 = ch < 3 ? 6 * ch : 18 + 5 * (ch - 3);
  const int rot1 = (ti + 2 * cg) & 7;
  const int rot3 = (tj + 2 * ch) & 7;
  v2u ep;
  { unsigned z = 0, so = WS_EP; asm volatile("buffer_load_dwordx2 %0, %1, %2, %3 offen sc1" : "=v"(ep) : "v"(z), "s"(rws), "s"(so) : "memory"); }
  const int piece = bid * 3 + w;
  const bool do_pt = w < 3 && piece < 688;
  const int pieced = bid + 256 * (w - 3);
  const bool do_pd = (w == 3 || w == 4) && pieced < 344;
  const unsigned PTS = w < 4 ? 12 * U + w * 4096 : 17 * U;
  unsigned qvoff, xvoff, nvoff;
  { int row = l >> 3, c = l & 7; int swz = ((row >> 1) & 1) | ((w & 1) << 1); qvoff = row * 3072 + ((c ^ (swz << 1)) * 16);
    int r16 = (w & 1) * 8 + row; xvoff = row * 1024 + ((c ^ ((r16 >> 1) & 7)) * 16);
    nvoff = row * 1024 + ((c ^ (((row >> 1) & 3) << 1)) * 16); }
  auto kvissue = [&](int n) {
    unsigned dst = L0 + (8 + n % 12) * U + w * 1024, so = (unsigned)((32 * ((n + 2 * rot1) & 15) + 8 * hd) * 3072 + (1 + team) * 1024 + cg * 128);
    DMA(rwq, dst, qvoff, so, 0);
  };
  auto qissue = [&](int m) {
    unsigned dst = L0 + (m < 5 ? m : m + 9) * U + w * 1024, so = (unsigned)((64 * ((m + rot1) & 7) + 8 * w) * 3072 + cg * 128);
    DMA(rwq, dst, qvoff, so, 0);
  };
  auto woissue = [&](int b) { unsigned dst = L0 + (12 + b) * U + w * 1024, so = (unsigned)((((b + rot3) & 7) * 8 + w) * 8192 + ch * 128); DMA(rwo, dst, nvoff, so, 0); };
#pragma unroll
  for (int c = 0; c < 8; c++) { unsigned dst = L0 + c * U + w * 1024, so = (unsigned)((ti * 64 + w * 8) * 1024 + ((c + rot1) & 7) * 128); DMA(rx, dst, xvoff, so, 0); }
  asm volatile("s_barrier" ::: "memory");
#pragma unroll
  for (int n = 0; n < 6; n++) kvissue(n);
  asm volatile("s_waitcnt vmcnt(6)" : "+v"(ep)::"memory");
  const unsigned long long epoch = ((unsigned long long)(unsigned)__builtin_amdgcn_readfirstlane(ep[1]) << 32) | (unsigned)__builtin_amdgcn_readfirstlane(ep[0]);
  v2u tag;
  { unsigned long long e = epoch * 0x9E3779B97F4A7C15ull + 0x632BE59BD9B4E019ull; tag[0] = (unsigned)e; tag[1] = (unsigned)(e >> 32); }
  const ldsp TB = L + 20 * U - 512;
  {
    float s = 0.f, q = 0.f;
#pragma unroll
    for (int c = 0; c < 8; c++) {
      v4u v = lds_r128(L + c * U + w * 1024 + v16);
#pragma unroll
      for (int j = 0; j < 4; j++) { s = dot_one(v[j], s); q = dot_sq(v[j], q); }
    }
    s = sum8(s); q = sum8(q);
    float rinv, mr; ln_scalars(s, q, rinv, mr);
    if ((l & 7) == 0) lds_w64(TB + (w * 8 + (l >> 3)) * 8, (v2u){__float_as_uint(rinv), __float_as_uint(mr)});
  }
  bf16x8 qf;
  v4u kk, vv;
  {
    const bf16x8 ones = asfrag((v4u){0x3f803f80u, 0x3f803f80u, 0x3f803f80u, 0x3f803f80u});
    f32x4 ak[2], av[2], ck, cv;
    ak[0] = ak[1] = av[0] = av[1] = ck = cv = (f32x4){0, 0, 0, 0};
    ldsp tb0, tb1, tbq;
    { int swz = ((q4 >> 1) & 1) | ((g & 1) << 1); tbq = L + g * 1024 + q4 * 128 + ((hd ^ swz) * 32) + p4 * 8; tb0 = tbq + 8 * U; tb1 = tbq + 14 * U; }
    const unsigned xo0 = (c16 >> 3) * 1024 + (c16 & 7) * 128 + ((g ^ (c16 >> 1)) * 16);
    const ldsp xb0 = L + team * 4096 + xo0, xb1 = L + team * 4096 + (xo0 ^ 64);
    bf16x8 xa[16][2], wf[3][2];
    v2u sqk[2]; f32x4 sv[2][2];
    auto rd = [&](int ks) {
      const int sl = ks % 12, b = ks & 1;
      const ldsp t = sl < 6 ? tb0 + sl * U : tb1 + (sl - 6) * U;
      const ldsp xp = (b ? xb1 : xb0) + (ks >> 1) * U;
      xa[ks][0] = asfrag(lds_r128(xp)); xa[ks][1] = asfrag(lds_r128(xp + 2048));
      wf[ks % 3][0] = trfrag(t, t + 512); wf[ks % 3][1] = trfrag(t + 4096, t + 4096 + 512);
    };
#pragma unroll
    for (int ks = 0; ks < 16; ks++) {
      const int b = ks % 3;
      if (ks == 0) {
        kvissue(6); kvissue(7);
        BAR_VM(2); rd(0); rd(1);
#pragma unroll
        for (int rt = 0; rt < 2; rt++) {
          sqk[rt] = lds_r64(TB + (team * 32 + rt * 16 + c16) * 8);
          sv[rt][0] = asf(lds_r128(TB + (team * 32 + rt * 16 + 4 * g) * 8)); sv[rt][1] = asf(lds_r128(TB + (team * 32 + rt * 16 + 4 * g) * 8 + 16));
        }
      }
      if (ks == 4) BAR_VM(1);
      if (ks == 8) BAR_VM(5);
      if (ks == 12) BAR_VM(8);
      SB();
      ak[0] = MFMA16(wf[b][0], xa[ks][0], ak[0]);
      SB();
      if (ks + 2 < 16) rd(ks + 2);
      SB();
      ak[1] = MFMA16(wf[b][0], xa[ks][1], ak[1]);
      av[0] = MFMA16(xa[ks][0], wf[b][1], av[0]);
      SB();
      if (ks == 0) { kvissue(8); kvissue(9); }
      if (ks == 1) kvissue(10);
      if (ks == 4) { kvissue(11); kvissue(12); }
      if (ks == 5) { kvissue(13); kvissue(14); }
      if (ks == 6) { kvissue(15); qissue(0); }
      if (ks == 7) { qissue(1); qissue(2); }
      if (ks == 8) qissue(3);
      if (ks == 9) qissue(4);
      if (ks == 10) {
  if (do_pt) {
    unsigned vo = (l >> 2) * 2752 + (l & 3) * 16, so = (unsigned)((piece & 15) * 32 * 2752 + (piece >> 4) * 64), dst = L0 + PTS;
    unsigned s2 = so + 16 * 2752, d2 = dst + 1024, d3 = dst + 2048, d4 = dst + 3072;
    DMA(rwg, dst, vo, so, 0); DMA(rwg, d2, vo, s2, 0); DMA(rwu, d3, vo, so, 0); DMA(rwu, d4, vo, s2, 0);
  }
  if (do_pd) {
    const int pc = pieced / 43, pk_ = pieced - 43 * pc;
#pragma unroll
    for (int kb = 0; kb < 4; kb++) { unsigned dst = L0 + PTS + kb * 1024, so = (unsigned)((pk_ * 4 + kb) * 8192 + pc * 128); DMA(rwd, dst, nvoff, so, 0); }
  }
        qissue(5);
      }
      if (ks == 11) { qissue(6); qissue(7); }
      if (ks == 13) {
  if (do_pt) {
    WAIT_VM(0);
    const ldsp a = L + PTS + (g & 1) * 2048 + (4 * (g >> 1) + q4) * 64 + p4 * 8;
    const unsigned p0 = (unsigned)(piece >> 4) * 2, kq = (unsigned)(piece & 15);
    float cs[2];
#pragma unroll
    for (int t = 0; t < 2; t++) {
      float c = 0.f;
#pragma unroll
      for (int s = 0; s < 2; s++) {
        v4u f = asu(trfrag(a + t * 32 + s * 512, a + t * 32 + s * 512 + 1024));
        unsigned so = WS_WT + ((p0 + t) * 32 + kq * 2 + s) * 1024;
        GSTORE_SC1(f, rws, v16, so);
#pragma unroll
        for (int j = 0; j < 4; j++) c = dot_one(f[j], c);
      }
      cs[t] = swap32_add(c, c);
    }
    unsigned cvv = __float_as_uint(l < 32 ? cs[0] : cs[1]), vo = (unsigned)((l >> 5) * 2048 + (l & 31) * 4), sc = WS_CP + (p0 * 16 + kq) * 128;
    ST32_SC1(cvv, rws, vo, sc);
  }
  if (do_pd) {
    WAIT_VM(0);
    const int pc = pieced / 43, pk_ = pieced - 43 * pc;
    const int row8 = 4 * (g & 1) + q4, swz8 = (row8 >> 1) & 3;
#pragma unroll
    for (int n = 0; n < 4; n++) {
      const ldsp a = L + PTS + (g >> 1) * 2048 + row8 * 128 + p4 * 8 + ((n ^ swz8) * 32);
      v4u f = asu(trfrag(a, a + 1024));
      unsigned so = WS_WD + (unsigned)((pc * 4 + n) * 44 + pk_) * 1024;
      GSTORE_SC1(f, rws, v16, so);
    }
  }
      }
      SB();
      av[1] = MFMA16(xa[ks][1], wf[b][1], av[1]);
      ck = MFMA16(wf[b][0], ones, ck);
      cv = MFMA16(ones, wf[b][1], cv);
      SB();
    }
    {
      v2u kp[2], vp[2];
#pragma unroll
      for (int rt = 0; rt < 2; rt++) {
        f32x4 kv = axmby(ak[rt], dup2(__uint_as_float(sqk[rt][0])), ck, dup2(__uint_as_float(sqk[rt][1])));
        kp[rt] = (v2u){pk(kv[0], kv[1]), pk(kv[2], kv[3])};
        const float c = cv[0];
        float v0 = av[rt][0] * sv[rt][0][0] - c * sv[rt][0][1], v1 = av[rt][1] * sv[rt][0][2] - c * sv[rt][0][3];
        float v2 = av[rt][2] * sv[rt][1][0] - c * sv[rt][1][1], v3 = av[rt][3] * sv[rt][1][2] - c * sv[rt][1][3];
        vp[rt] = (v2u){pk(v0, v1), pk(v2, v3)};
      }
      kk = join16(kp[0], kp[1]); vv = join32(vp[0], vp[1]);
      unsigned so = WS_KV + (unsigned)((ti * 2 + team) * 32 + cg * 4 + hd) * 2048, s2 = so + 1024;
      if (!(do_pt || do_pd)) WAIT_VM(0);
      GSTORE_SC1(kk, rws, v16, so); GSTORE_SC1(vv, rws, v16, s2);
    }
    f32x4 aq[2], cq;
    aq[0] = aq[1] = cq = (f32x4){0, 0, 0, 0};
    bf16x8 qw[4];
    auto rdq = [&](int ks) { const ldsp t = (ks < 10 ? tbq + (ks >> 1) * U : tb1 + ((ks >> 1) - 5) * U) + (ks & 1) * 4096; qw[ks & 3] = trfrag(t, t + 512); };
    BAR_LDS();
    rdq(10); rdq(11); rdq(12);
#pragma unroll
    for (int i = 0; i < 16; i++) {
      const int ks = (i + 10) & 15;
      if (i == 6) {
        BAR_VM(0);
        if (w == 0) { unsigned z = 0, so = FL_K + (unsigned)bid * 128; ST64_SC1(tag, rws, z, so); }
      }
      SB();
      aq[0] = MFMA16(qw[ks & 3], xa[ks][0], aq[0]);
      SB();
      if (i + 3 < 16) rdq((ks + 3) & 15);
      if (i >= 6 && i < 14) woissue(i - 6);
      SB();
      aq[1] = MFMA16(qw[ks & 3], xa[ks][1], aq[1]);
      cq = MFMA16(qw[ks & 3], ones, cq);
      SB();
    }
    const float QS = 0.25f * 1.4426950408889634f;
    v2u qp[2];
#pragma unroll
    for (int rt = 0; rt < 2; rt++) {
      f32x4 qv = axmby(aq[rt], dup2(__uint_as_float(sqk[rt][0]) * QS), cq, dup2(__uint_as_float(sqk[rt][1]) * QS));
      qp[rt] = (v2u){pk(qv[0], qv[1]), pk(qv[2], qv[3])};
    }
    qf = asfrag(join16(qp[0], qp[1]));
  }
  const unsigned CONST = 11 * U, SY = L0 + 11 * U + 1024;
  { const ldsp kvs = L + (9 + team) * U + hd * 2048 + v16; lds_w128(kvs, kk); lds_w128(kvs + 1024, vv); }
  if (tid >= 64 && tid < 68) { unsigned v = (tid & 1) ? 0u : 0x3f803f80u; lds_w128(L + CONST + (tid & 1) * 16 + ((tid >> 1) & 1) * 512, (v4u){v, v, v, v}); }
  if (tid >= 128 && tid < 136) lds_w128(L + CONST + 1024 + (tid - 128) * 16, (v4u){0, 0, 0, 0});
  if (w == 0) {
    int tp = ti - 4 + (l & 3); tp = tp < 0 ? ti : tp;
    const int pb = ((tp >> 3) << 1) + (cg >> 2) + ((cg & 3) << 3) + ((tp & 7) << 5);
    wait_flags(rws, (unsigned)pb * 128, FL_K, tag);
  }
  BAR_LDS();
  {
    const int Tg = 2 * ti - 8 + w;
    unsigned s2 = Tg >= 0 ? WS_KV + (unsigned)(Tg * 32 + cg * 4) * 2048 : 0x7ff00000u, d2 = L0 + (1 + w) * U;
    DMA_SC1(rws, d2, v16, s2, 0); DMA_SC1(rws, d2, v16, s2, 1024); DMA_SC1(rws, d2, v16, s2, 2048); DMA_SC1(rws, d2, v16, s2, 3072);
    s2 += 4096; d2 += 4096;
    DMA_SC1(rws, d2, v16, s2, 0); DMA_SC1(rws, d2, v16, s2, 1024); DMA_SC1(rws, d2, v16, s2, 2048); DMA_SC1(rws, d2, v16, s2, 3072);
    { unsigned xv = (l >> 3) * 1024 + (l & 7) * 16; unsigned so = (unsigned)((tj * 64 + w * 8) * 1024 + ch * 128); unsigned dst = L0 + w * 1024; DMA(rx, dst, xv, so, 0); }
  }
  v4u wq[32];
  const bool two = 8 + w < 2 * nks;
  const unsigned wt0 = WS_WT + (unsigned)(2 * ks0 + w) * 32768, wt1 = wt0 + 8 * 32768;
  auto wload4 = [&](int n, unsigned base, unsigned cond) {
    unsigned so = base + (unsigned)((n + 4 * rot3) & 31) * 1024;
    cond = (unsigned)__builtin_amdgcn_readfirstlane((int)cond);
    asm volatile("s_cmp_eq_u32 %7, 0\n\ts_cbranch_scc1 1f\n\t"
                 "buffer_load_dwordx4 %0, %4, %5, %6 offen sc1\n\tbuffer_load_dwordx4 %1, %4, %5, %6 offen offset:1024 sc1\n\t"
                 "buffer_load_dwordx4 %2, %4, %5, %6 offen offset:2048 sc1\n\tbuffer_load_dwordx4 %3, %4, %5, %6 offen offset:3072 sc1\n1:"
                 : "+v"(wq[n]), "+v"(wq[n + 1]), "+v"(wq[n + 2]), "+v"(wq[n + 3]) : "v"(v16), "s"(rws), "s"(so), "s"(cond) : "memory", "scc");
  };

#pragma unroll
  for (int n = 0; n < 32; n++) asm volatile("" : "=v"(wq[n]));
  BAR_VM(0);
  if (w == 5) {
    int pb = (16 * ks0) / 3 + (l < 33 ? l : 0); const int pe = (16 * (ks0 + nks) - 1) / 3; pb = pb > pe ? pe : pb;
    wait_flags(rws, (unsigned)pb * 128, FL_K, tag);
    lds_inc(SY + 18 * 4);
  }
  if (w == 6) {
    const int pb = (43 * ch + (l < 43 ? l : 0)) & 255;
    wait_flags(rws, (unsigned)pb * 128, FL_K, tag);
    lds_inc(SY + 18 * 4);
  }
  const int r32 = l & 31, h = l >> 5;
  {
    const int qt = team;
    const int nskip = max(0, 8 - (2 * ti + qt));
    const ldsp kbase = L + (1 + qt) * U + hd * 2048 + v16;
    f32x16 zero;
#pragma unroll
    for (int r = 0; r < 16; r++) zero[r] = 0.f;
    const int qq = r32 - 4 * h;
    const unsigned vstride = (r32 < 16) ? U : 0u;
    const ldsp va = (r32 < 16) ? (L + (1 + qt) * U + hd * 2048 + 1024 + (h * 16 + r32) * 16) : (L + CONST + ((r32 == 16 || r32 == 20) ? 0u : 16u));
    f32x16 o = zero, sm;
    float m;
    auto bias = [&](int kt) -> float { return kt >= nskip ? 0.f : -3.0e38f; };
    auto kfr = [&](int kt) -> bf16x8 { return asfrag(kt < 8 ? lds_r128(kbase + kt * U) : lds_r128(kbase + 4 * U + 4 * U)); };
    {
      bf16x8 k8 = kfr(8), k0 = kfr(0), k1 = kfr(1), k2 = kfr(2), k3 = kfr(3), k4 = kfr(4);
      SB();
      f32x16 sa = MFMA32(k8, qf, zero), sb = MFMA32(k0, qf, zero), sc = MFMA32(k1, qf, zero);
      bf16x8 k5 = kfr(5), k6 = kfr(6), k7 = kfr(7);
      SB();
      const float b0 = bias(0);
#pragma unroll
      for (int r = 0; r < 16; r++) { const int kc = (r & 3) + 8 * (r >> 2); sm[r] = (qq >= kc) ? sa[r] : sb[r] + b0; }
      sa = MFMA32(k2, qf, zero); sb = MFMA32(k3, qf, zero);
      m = tmax(sm, -3.0e38f);
      m = __builtin_fmaxf(m, tmax(sc, -3.0e38f) + bias(1));
      sc = MFMA32(k4, qf, zero);
      m = __builtin_fmaxf(m, tmax(sa, -3.0e38f) + bias(2));
      sa = MFMA32(k5, qf, zero);
      m = __builtin_fmaxf(m, tmax(sb, -3.0e38f) + bias(3));
      sb = MFMA32(k6, qf, zero);
      m = __builtin_fmaxf(m, tmax(sc, -3.0e38f) + bias(4));
      sc = MFMA32(k7, qf, zero);
      m = __builtin_fmaxf(m, tmax(sa, -3.0e38f) + bias(5));
      m = __builtin_fmaxf(m, tmax(sb, -3.0e38f) + bias(6));
      m = __builtin_fmaxf(m, tmax(sc, -3.0e38f) + bias(7));
    }
    { auto rr = __builtin_amdgcn_permlane32_swap(__float_as_uint(m), __float_as_uint(m), false, false); m = __builtin_fmaxf(__uint_as_float(rr[0]), __uint_as_float(rr[1])); }
    f32x16 negm;
#pragma unroll
    for (int r = 0; r < 16; r++) negm[r] = -m;
    bf16x8 kn = kfr(2);
    f32x16 sc = MFMA32(kfr(1), qf, negm), sn;
    v4u vf[2][2];
    {
      ldsp v8 = va + 8 * vstride;
      v4u v80 = lds_r128(v8), v81 = lds_r128(v8 + 512), v00 = lds_r128(va), v01 = lds_r128(va + 512);
      vf[1][0] = lds_r128(va + vstride); vf[1][1] = lds_r128(va + vstride + 512);
#pragma unroll
      for (int r = 0; r < 16; r++) sm[r] -= m;
      v4u p0, p1; pexp(sm, p0, p1);
      v4u a0, a1, b0, b1;
#pragma unroll
      for (int j = 0; j < 4; j++) {
        const int kc0 = ((2 * j) & 3) + 8 * ((2 * j) >> 2), kc1 = kc0 + 16;
        unsigned m0 = (qq > kc0) ? 0xffffffffu : ((qq == kc0) ? 0xffffu : 0u);
        unsigned m1 = (qq > kc1) ? 0xffffffffu : ((qq == kc1) ? 0xffffu : 0u);
        a0[j] = p0[j] & m0; b0[j] = p0[j] ^ a0[j]; a1[j] = p1[j] & m1; b1[j] = p1[j] ^ a1[j];
      }
      o = MFMA32(asfrag(v80), asfrag(a0), o); o = MFMA32(asfrag(v81), asfrag(a1), o);
      o = MFMA32(asfrag(v00), asfrag(b0), o); o = MFMA32(asfrag(v01), asfrag(b1), o);
    }
    lds_wait(SY + 18 * 4, 2);
    wload4(0, wt0, 1u); wload4(4, wt0, 1u);
#pragma unroll
    for (int kt = 1; kt < 8; kt++) {
      if (kt < 7) {
        sn = MFMA32(kn, qf, negm);
        if (kt < 6) kn = kfr(kt + 2);
        ldsp vn = va + (kt + 1) * vstride;
        vf[(kt + 1) & 1][0] = lds_r128(vn); vf[(kt + 1) & 1][1] = lds_r128(vn + 512);
      }
      if (kt <= 2) wload4(4 * kt + 4, wt0, 1u); else if (kt <= 5) wload4(4 * kt + 4, wt0, (0xF1u >> w) & 1u);
      if (kt == 5) wload4(28, wt0, (0xF1u >> w) & 1u);
      SB();
      if (kt >= nskip) {
        v4u p0, p1; pexp(sc, p0, p1);
        o = MFMA32(asfrag(vf[kt & 1][0]), asfrag(p0), o); o = MFMA32(asfrag(vf[kt & 1][1]), asfrag(p1), o);
      }
      SB();
      sc = sn;
    }
    float inv = __builtin_amdgcn_rcpf(o[8]);
    v4u ov;
#pragma unroll
    for (int j = 0; j < 4; j++) ov[j] = pk(o[2 * j] * inv, o[2 * j + 1] * inv);
    unsigned vo = (unsigned)((r32 >> 4) * 16384 + ((2 * (hd & 1) + h) * 16 + c16) * 16);
    unsigned so = WS_O + (unsigned)((ti * 4 + qt * 2) * 16 + cg * 2 + (hd >> 1)) * 1024;
    GSTORE_SC1(ov, rws, vo, so);
  }
  lds_inc(SY + 10 * 4);
  const ldsp CSW = L + 11 * U + 2048 + w * 256;
  {
    WAIT_VM(0);
    if (lds_inc(SY + team * 4) == 3) { unsigned z = 0, so = FL_O + (unsigned)((ti * 2 + team) * 8 + cg) * 128; ST64_SC1(tag, rws, z, so); }
  }
#pragma unroll
  for (int n = 16; n < 32; n += 4) wload4(n, wt0, (0x0Eu >> w) & 1u);
  if (bid == 0 && w == 7) {
#pragma unroll 1
    for (int k = 0; k < 4; k++) wait_flags(rws, (unsigned)l * 128, FL_K + (unsigned)k * 8192, tag);
    unsigned long long e = epoch + 1ull; v2u en = {(unsigned)e, (unsigned)(e >> 32)}; unsigned z = 0, so = WS_EP; ST64_SC1(en, rws, z, so);
  }
  const int rt3 = team * 2 + ((w >> 1) & 1), nh = w & 1;
  const int row8 = 4 * (g & 1) + q4, swz8 = (row8 >> 1) & 3;
  {
    bf16x8 wf[3][4];
    ldsp tb[4];
#pragma unroll
    for (int n = 0; n < 4; n++) tb[n] = L + 12 * U + nh * 32768 + (g >> 1) * 2048 + row8 * 128 + p4 * 8 + ((n ^ swz8) * 32);
    auto rdw = [&](int k) {
#pragma unroll
      for (int n = 0; n < 4; n++) wf[k % 3][n] = trfrag(tb[n] + k * 4096, tb[n] + k * 4096 + 1024);
    };
    rdw(0); rdw(1); rdw(2);
    const ldsp xa = L + (rt3 * 16 + c16) * 128 + nh * 64 + g * 8;
    v2u x0 = lds_r64(xa), x1 = lds_r64(xa + 32);
    if (hd == 0) { lds_wait(SY + team * 4, 4); wait_flags(rws, (unsigned)(l & 7) * 128, FL_O + (unsigned)((tj * 2 + team) * 8) * 128, tag); lds_inc(SY + (2 + team) * 4); }
    else lds_wait(SY + (2 + team) * 4, 1);
    v4u of[8];
#pragma unroll
    for (int k = 0; k < 8; k++) { unsigned so = WS_O + (unsigned)((tj * 4 + rt3) * 16 + ((nh * 8 + k + 2 * rot3) & 15)) * 1024; GLOAD_SC1(of[k], rws, v16, so); }
    f32x4 acc[4];
#pragma unroll
    for (int n = 0; n < 4; n++) acc[n] = (f32x4){0, 0, 0, 0};
#define WO_STEP(k, N) asm volatile("s_waitcnt vmcnt(" #N ")" : "+v"(of[k])::"memory"); SB(); \
    _Pragma("unroll") for (int n = 0; n < 4; n++) acc[n] = MFMA16(wf[(k) % 3][n], asfrag(of[k]), acc[n]); SB(); \
    if ((k) + 3 < 8) rdw((k) + 3); SB();
    WO_STEP(0, 7) WO_STEP(1, 6) WO_STEP(2, 5) WO_STEP(3, 4) WO_STEP(4, 3) WO_STEP(5, 2) WO_STEP(6, 1) WO_STEP(7, 0)
    const ldsp ex = L + (team ? 10 : 1) * U + v16;
    {
      f32x4 s0 = nh ? acc[0] : acc[2], s1 = nh ? acc[1] : acc[3];
      lds_w128(ex + hd * 2048, __builtin_bit_cast(v4u, s0)); lds_w128(ex + hd * 2048 + 1024, __builtin_bit_cast(v4u, s1));
    }
    lds_inc(SY + (24 + w) * 4);
    lds_wait(SY + (24 + (w ^ 1)) * 4, 1);
    const f32x4 a0 = (nh ? acc[2] : acc[0]) + asf(lds_r128(ex + (hd ^ 1) * 2048)), a1 = (nh ? acc[3] : acc[1]) + asf(lds_r128(ex + (hd ^ 1) * 2048 + 1024));
    v4u hv;
    hv[0] = pk(lo16(x0[0]) + bfr(a0[0]), hi16(x0[0]) + bfr(a0[1])); hv[1] = pk(lo16(x0[1]) + bfr(a0[2]), hi16(x0[1]) + bfr(a0[3]));
    hv[2] = pk(lo16(x1[0]) + bfr(a1[0]), hi16(x1[0]) + bfr(a1[1])); hv[3] = pk(lo16(x1[1]) + bfr(a1[2]), hi16(x1[1]) + bfr(a1[3]));
    unsigned vo = (unsigned)((g >> 1) * 1024 + (g & 1) * 512 + ((rt3 & 1) * 16 + c16) * 16);
    unsigned so = WS_H + (unsigned)((tj * 2 + team) * 32 + (2 * ch + nh) * 2) * 1024;
    GSTORE_SC1(hv, rws, vo, so);
    float s = 0.f, q = 0.f;
#pragma unroll
    for (int j = 0; j < 4; j++) { s = dot_one(hv[j], s); q = dot_sq(hv[j], q); }
    s = sum_g(s); q = sum_g(q);
    v2u sp = {__float_as_uint(s), __float_as_uint(q)};
    unsigned vs = g == 0 ? (unsigned)(((rt3 & 1) * 16 + c16) * 8) : OOB, ss = WS_SP + (unsigned)((tj * 2 + team) * 16 + 2 * ch + nh) * 256;
    ST64_SC1(sp, rws, vs, ss);
  }
  lds_inc(SY + 11 * 4);
  WAIT_VM(0);
  if (lds_inc(SY + (4 + team) * 4) == 3) { unsigned z = 0, so = FL_H + (unsigned)((tj * 2 + team) * 8 + ch) * 128; ST64_SC1(tag, rws, z, so); }
  {
    v4u cp[4];
#pragma unroll
    for (int t = 0; t < 4; t++) { unsigned so = WS_CP + (unsigned)(2 * ks0 + w + ((t >> 1) && two ? 8 : 0)) * 2048 + (t & 1) * 1024; GLOAD_SC1(cp[t], rws, v16, so); }
    asm volatile("s_waitcnt vmcnt(0)" : "+v"(cp[0]), "+v"(cp[1]), "+v"(cp[2]), "+v"(cp[3])::"memory");
#pragma unroll
    for (int ps = 0; ps < 2; ps++) {
      f32x4 c = asf(cp[2 * ps]) + asf(cp[2 * ps + 1]);
#pragma unroll
      for (int j = 0; j < 4; j++) c[j] = sum_g(dpp_add<0x128>(c[j]));
      if (l < 8) lds_w128(CSW + ps * 128 + l * 16, __builtin_bit_cast(v4u, c));
    }
  }
  auto wdf = [&](int n) -> v4u& { return wq[n]; };
  auto dload6 = [&](int q, unsigned cond) {
    const int c = (rot3 + (team ? 0 : 4) + q) & 7;
    unsigned so = WS_WD + (unsigned)((ch * 4 + hd) * 44 + (c < 3 ? 6 * c : 5 * c + 3)) * 1024, s2 = so + 4096;
    cond = (unsigned)__builtin_amdgcn_readfirstlane((int)cond);
    asm volatile("s_cmp_eq_u32 %10, 0\n\ts_cbranch_scc1 1f\n\t"
                 "buffer_load_dwordx4 %0, %6, %7, %8 offen sc1\n\tbuffer_load_dwordx4 %1, %6, %7, %8 offen offset:1024 sc1\n\t"
                 "buffer_load_dwordx4 %2, %6, %7, %8 offen offset:2048 sc1\n\tbuffer_load_dwordx4 %3, %6, %7, %8 offen offset:3072 sc1\n\t"
                 "buffer_load_dwordx4 %4, %6, %7, %9 offen sc1\n\tbuffer_load_dwordx4 %5, %6, %7, %9 offen offset:1024 sc1\n1:"
                 : "+v"(wdf(6 * q)), "+v"(wdf(6 * q + 1)), "+v"(wdf(6 * q + 2)), "+v"(wdf(6 * q + 3)), "+v"(wdf(6 * q + 4)), "+v"(wdf(6 * q + 5))
                 : "v"(v16), "s"(rws), "s"(so), "s"(s2), "s"(cond) : "memory", "scc");
  };
  const unsigned STG = 11 * 4096 + hd * 8192;
  if (two) {
    lds_wait(SY + 10 * 4, 8);
#pragma unroll
    for (int n = 0; n < 8; n += 4) {
      unsigned so = wt1 + (unsigned)((n + 4 * rot3) & 31) * 1024, dst = L0 + STG + n * 1024;
      DMA_SC1(rws, dst, v16, so, 0); DMA_SC1(rws, dst, v16, so, 1024); DMA_SC1(rws, dst, v16, so, 2048); DMA_SC1(rws, dst, v16, so, 3072);
    }
  }
  auto lload4 = [&](int n, unsigned cond) {
    const unsigned sa = L0 + STG + n * 1024 + v16;
    cond = (unsigned)__builtin_amdgcn_readfirstlane((int)cond);
    asm volatile("s_cmp_eq_u32 %5, 0\n\ts_cbranch_scc1 1f\n\t"
                 "ds_read_b128 %0, %4\n\tds_read_b128 %1, %4 offset:1024\n\tds_read_b128 %2, %4 offset:2048\n\tds_read_b128 %3, %4 offset:3072\n\ts_waitcnt lgkmcnt(0)\n1:"
                 : "+v"(wq[n]), "+v"(wq[n + 1]), "+v"(wq[n + 2]), "+v"(wq[n + 3]) : "v"(sa), "s"(cond) : "memory", "scc");
  };
  auto fetch_h = [&]() {
    if (hd == 1) { lds_wait(SY + (4 + team) * 4, 4); wait_flags(rws, (unsigned)(l & 7) * 128, FL_H + (unsigned)((tj * 2 + team) * 8) * 128, tag); lds_wait(SY + (10 + team) * 4, 8); lds_inc(SY + (6 + team) * 4); }
    else lds_wait(SY + (6 + team) * 4, 1);
    unsigned s2 = WS_H + (unsigned)((tj * 2 + team) * 32 + ((hd * 8 + 4 * rot3) & 31)) * 1024, d2 = L0 + (1 + 11 * team + hd) * U;
    DMA_SC1(rws, d2, v16, s2, 0); DMA_SC1(rws, d2, v16, s2, 1024); DMA_SC1(rws, d2, v16, s2, 2048); DMA_SC1(rws, d2, v16, s2, 3072);
    s2 = WS_H + (unsigned)((tj * 2 + team) * 32 + ((hd * 8 + 4 + 4 * rot3) & 31)) * 1024; d2 += 4096;
    DMA_SC1(rws, d2, v16, s2, 0); DMA_SC1(rws, d2, v16, s2, 1024); DMA_SC1(rws, d2, v16, s2, 2048); DMA_SC1(rws, d2, v16, s2, 3072);
    unsigned s3 = WS_SP + (unsigned)((tj * 2 + team) * 4 + hd) * 1024, d3 = L0 + (team ? 1 : 10) * 4096 + hd * 1024;
    DMA_SC1(rws, d3, v16, s3, 0);
  };
  const ldsp hb = L + U + v16;
  float lri[2], lmr[2];
  auto scalars = [&](int t) {
    const ldsp sp = L + (t ? 1 : 10) * 4096 + h * 2048 + r32 * 8;
    float s = 0.f, q = 0.f;
#pragma unroll
    for (int i = 0; i < 8; i++) { v2u v = lds_r64(sp + i * 256); s += __uint_as_float(v[0]); q += __uint_as_float(v[1]); }
    ln_scalars(swap32_add(s, s), swap32_add(q, q), lri[t], lmr[t]);
  };
  auto glu_store = [&](const f32x16& a, int t, int ps) {
    const f32x2 ri = dup2(lri[t]), mr = dup2(lmr[t]), rg_ = dup2(lri[t] * -1.4426950408889634f), mg_ = dup2(lmr[t] * -1.4426950408889634f);
    v4u o;
#pragma unroll
    for (int qd = 0; qd < 2; qd++) {
      f32x4 cgv = asf(lds_r128(CSW + ps * 128 + (2 * qd + h) * 16)), cuv = asf(lds_r128(CSW + ps * 128 + 64 + (2 * qd + h) * 16));
      f32x4 gg = axmby((f32x4){a[4 * qd], a[4 * qd + 1], a[4 * qd + 2], a[4 * qd + 3]}, rg_, cgv, mg_);
      f32x4 uu = axmby((f32x4){a[8 + 4 * qd], a[9 + 4 * qd], a[10 + 4 * qd], a[11 + 4 * qd]}, ri, cuv, mr);
      float v[4];
#pragma unroll
      for (int j = 0; j < 4; j++) v[j] = uu[j] * __builtin_amdgcn_rcpf(1.f + __builtin_amdgcn_exp2f(gg[j]));
      o[2 * qd] = pk(v[0], v[1]); o[2 * qd + 1] = pk(v[2], v[3]);
    }
    unsigned so = WS_ACT + (unsigned)((tj * 2 + t) * 86 + 2 * ks0 + w + 8 * ps) * 1024;
    GSTORE_SC1(o, rws, v16, so);
  };
  auto aflag = [&](int cls, unsigned n) {
    if (lds_inc(SY + (12 + cls) * 4) == n - 1) { unsigned z = 0, so = FL_A + (unsigned)((tj * 4 + cls) * 8 + ch) * 128; ST64_SC1(tag, rws, z, so); }
  };
  v4u hres;
  {
    fetch_h();
    if (team == 0) WAIT_VM(0); else WAIT_VM(9);
    if (team == 0) lds_inc(SY + 8 * 4);
    lds_wait(SY + 8 * 4, 4);
    const int npass = two ? 2 : 1;
    const ldsp hb2 = hb + 11 * U;
    f32x4 ecg, ecu; v4u eo; float ev0;
    auto epi = [&](int k, const f32x16& a, int t, int ps, unsigned so) {
      if (k == 0 || k == 9) { const int qd = k ? 1 : 0; ecg = asf(lds_r128(CSW + ps * 128 + (2 * qd + h) * 16)); ecu = asf(lds_r128(CSW + ps * 128 + 64 + (2 * qd + h) * 16)); }
      const int e = k < 9 ? (k - 2) / 2 : 4 + (k - 11) / 2;
      if ((k >= 2 && k <= 8 && (k & 1) == 0) || (k >= 11 && k <= 17 && (k & 1) == 1)) {
        const int j = e & 3;
        float gg = (a[e] * lri[t] - ecg[j] * lmr[t]) * -1.4426950408889634f, uu = a[8 + e] * lri[t] - ecu[j] * lmr[t];
        float v = uu * __builtin_amdgcn_rcpf(1.f + __builtin_amdgcn_exp2f(gg));
        if (e & 1) eo[e >> 1] = pk(ev0, v); else ev0 = v;
      }
      if (k == 18) GSTORE_SC1(eo, rws, v16, so);
    };
    const unsigned aso0 = WS_ACT + (unsigned)(tj * 2 * 86 + 2 * ks0 + w) * 1024;
    f32x16 accA, accB;
    const unsigned more = (unsigned)(npass - 1);
    {
#pragma unroll
      for (int r = 0; r < 16; r++) { accA[r] = 0.f; accB[r] = 0.f; }
      v4u hf[4];
      hf[0] = lds_r128(hb); hf[1] = lds_r128(hb + 1024); hf[2] = lds_r128(hb + 2048);
#pragma unroll
      for (int k = 0; k < 32; k++) {
        if (k + 3 < 32) hf[(k + 3) & 3] = lds_r128(hb + (k + 3) * 1024);
        SB();
        accA = MFMA32(asfrag(wq[k]), asfrag(hf[k & 3]), accA);
        SB();
        if (k == 8) scalars(0);
        if (k == 22 && team) { WAIT_VM(0); lds_inc(SY + 9 * 4); }
        SB();
      }
      lds_wait(SY + 9 * 4, 4);
      hf[0] = lds_r128(hb2); hf[1] = lds_r128(hb2 + 1024); hf[2] = lds_r128(hb2 + 2048);
#pragma unroll
      for (int k = 0; k < 32; k++) {
        if (k + 3 < 32) hf[(k + 3) & 3] = lds_r128(hb2 + (k + 3) * 1024);
        SB();
        accB = MFMA32(asfrag(wq[k]), asfrag(hf[k & 3]), accB);
        SB();
        epi(k, accA, 0, 0, aso0);
        if (k == 22) scalars(1);
        if ((k & 3) == 3) { if (k < 8) lload4(k - 3, more); else wload4(k - 3, wt1, more); }
        SB();
      }
    }
    if (!two) { glu_store(accB, 1, 0); WAIT_VM(0); aflag(1, 8); }
    dload6(0, 1u - more); dload6(1, 1u - more); dload6(2, 1u - more); dload6(3, 1u - more);
    if (two) {
      f32x16 acc2;
#pragma unroll
      for (int r = 0; r < 16; r++) { accA[r] = 0.f; acc2[r] = 0.f; }
      v4u hf[3], hg[3];
      hf[0] = lds_r128(hb); hg[0] = lds_r128(hb2); hf[1] = lds_r128(hb + 1024); hg[1] = lds_r128(hb2 + 1024);
#pragma unroll
      for (int k = 0; k < 32; k++) {
        if (k + 2 < 32) { hf[(k + 2) % 3] = lds_r128(hb + (k + 2) * 1024); hg[(k + 2) % 3] = lds_r128(hb2 + (k + 2) * 1024); }
        switch (k) {
#define WV(K, N) case K: asm volatile("s_waitcnt vmcnt(" #N ")" : "+v"(wq[K]), "+v"(wq[K + 1]), "+v"(wq[K + 2]), "+v"(wq[K + 3])::"memory"); break;
          WV(8, 20) WV(12, 16)
          default: break;
        }
        if (k == 16) {
          asm volatile("s_waitcnt vmcnt(0)" : "+v"(wq[16]), "+v"(wq[17]), "+v"(wq[18]), "+v"(wq[19]), "+v"(wq[20]), "+v"(wq[21]), "+v"(wq[22]), "+v"(wq[23]), "+v"(wq[24]),
                       "+v"(wq[25]), "+v"(wq[26]), "+v"(wq[27]), "+v"(wq[28]), "+v"(wq[29]), "+v"(wq[30]), "+v"(wq[31])::"memory");
          aflag(1, 8);
          dload6(0, 1u); dload6(1, 1u);
        }
        SB();
        accA = MFMA32(asfrag(wq[k]), asfrag(hf[k % 3]), accA);
        SB();
        if (k < 10) epi(2 * k, accB, 1, 0, aso0 + 86 * 1024);
        SB();
        acc2 = MFMA32(asfrag(wq[k]), asfrag(hg[k % 3]), acc2);
        SB();
        if (k < 9) epi(2 * k + 1, accB, 1, 0, aso0 + 86 * 1024);
        if (k == 19) dload6(2, 1u);
        if (k == 23) dload6(3, 1u);
        SB();
      }
      glu_store(accA, 0, 1); glu_store(acc2, 1, 1);
      WAIT_VM(0);
      aflag(2, (unsigned)(2 * nks - 8));
    }
    {
      const ldsp hp = L + (1 + team * 11) * U + ((((2 * ch + (hd >> 1)) * 2 - 4 * rot3) & 31) + (g >> 1)) * 1024 + (g & 1) * 512 + c16 * 16 + (hd & 1) * 8;
      v2u r0 = lds_r64(hp), r1 = lds_r64(hp + 256);
      hres = (v4u){r0[0], r0[1], r1[0], r1[1]};
    }
  }
  {
    int l5 = threadIdx.x & 63; asm volatile("" : "+v"(l5));
    const int l = l5, c16 = l & 15, g = l >> 4;
    const unsigned v16 = l * 16;
    const unsigned abase = WS_ACT + (unsigned)(tj * 2 * 86) * 1024;
    auto aissue = [&](int ks, unsigned slot) {
      unsigned so = abase + (unsigned)ks * 2048, dst = L0 + slot * 4096;
      DMA_SC1(rws, dst, v16, so, 0); DMA_SC1(rws, dst, v16, so, 1024);
      so += 86 * 1024; dst += 2048;
      DMA_SC1(rws, dst, v16, so, 0); DMA_SC1(rws, dst, v16, so, 1024);
    };
    auto cbase = [&](int q) -> int { return q == 0 ? 32 : (q == 1 ? 11 : (q == 2 ? 2 : 24)); };
    f32x4 acc[4];
#pragma unroll
    for (int r = 0; r < 4; r++) acc[r] = (f32x4){0, 0, 0, 0};
    const ldsp ap = L + (g >> 1) * 1024 + (g & 1) * 512 + c16 * 16;
    v4u af[2][4];
    auto rda = [&](ldsp b, int buf) {
#pragma unroll
      for (int r = 0; r < 4; r++) af[buf][r] = lds_r128(b + (r >> 1) * 2048 + (r & 1) * 256);
    };
    auto steps4 = [&](int q) {
      const ldsp b = ap + (cbase(q) + (team ? 0 : 4)) * 4096;
      rda(b, 0);
#pragma unroll
      for (int i = 0; i < 4; i++) {
        if (i < 3) rda(b + (i + 1) * 4096, (i + 1) & 1);
        SB();
#pragma unroll
        for (int r = 0; r < 4; r++) acc[r] = MFMA16(asfrag(wdf(6 * q + i)), asfrag(af[i & 1][r]), acc[r]);
        SB();
      }
    };
    if (team == 1) {
      if (w == 4) { lds_wait(SY + 13 * 4, 8); wait_flags(rws, (unsigned)(l & 7) * 128, FL_A + (unsigned)((tj * 4 + 1) * 8) * 128, tag); lds_inc(SY + 15 * 4); }
      else lds_wait(SY + 15 * 4, 1);
#pragma unroll
      for (int q = 0; q < 2; q++) { aissue(early_rt(4 * q + hd, rot3), (unsigned)(cbase(q) + hd)); aissue(early_rt(16 + 4 * q + hd, rot3), (unsigned)(cbase(q) + 4 + hd)); }
    }
    BAR_LDS();
    aissue(early_rt((team ? 8 : 24) + hd, rot3), (unsigned)(cbase(2) + (team ? 0 : 4) + hd));
    aissue(early_rt((team ? 12 : 28) + hd, rot3), (unsigned)(cbase(3) + (team ? 0 : 4) + hd));
    BAR_VM(8);
#pragma unroll
    for (int n = 0; n < 24; n += 3) asm volatile("" : "+v"(wq[n]), "+v"(wq[n + 1]), "+v"(wq[n + 2]));
    steps4(0); steps4(1);
    if (w == 0) wait_flags(rws, (unsigned)(l & 7) * 128, FL_A + (unsigned)((tj * 4 + 2) * 8) * 128, tag);
    BAR_VM(0);
    {
      const int c = (rot3 + w) & 7, k0 = (c < 3 ? 6 * c : 5 * c + 3) + 4;
      const unsigned sl = (unsigned)((w < 4 ? 32 : 11) + 2 * hd);
      aissue(k0, sl);
      if (c < 3) aissue(k0 + 1, sl + 1);
    }
    steps4(2); steps4(3);
    BAR_VM(0);
    {
      const ldsp b = ap + (team ? 32 : 11) * 4096;
#pragma unroll
      for (int q = 0; q < 4; q++) {
        const int c = (rot3 + (team ? 0 : 4) + q) & 7;
        rda(b + 2 * q * 4096, 0);
        if (c < 3) {
          rda(b + (2 * q + 1) * 4096, 1);
#pragma unroll
          for (int r = 0; r < 4; r++) acc[r] = MFMA16(asfrag(wdf(6 * q + 5)), asfrag(af[1][r]), acc[r]);
        }
#pragma unroll
        for (int r = 0; r < 4; r++) acc[r] = MFMA16(asfrag(wdf(6 * q + 4)), asfrag(af[0][r]), acc[r]);
      }
    }
    {
      f32x4 s0 = team ? acc[0] : acc[2], s1 = team ? acc[1] : acc[3];
      lds_w128(L + 2 * 4096 + w * 2048 + v16, __builtin_bit_cast(v4u, s0));
      lds_w128(L + 2 * 4096 + w * 2048 + 1024 + v16, __builtin_bit_cast(v4u, s1));
    }
    BAR_LDS();
    {
      f32x4 r0 = asf(lds_r128(L + 2 * 4096 + (w ^ 4) * 2048 + v16)), r1 = asf(lds_r128(L + 2 * 4096 + (w ^ 4) * 2048 + 1024 + v16));
      f32x4 a0 = (team ? acc[2] : acc[0]) + r0, a1 = (team ? acc[3] : acc[1]) + r1;
      v2u o0 = {pk(lo16(hres[0]) + bfr(a0[0]), hi16(hres[0]) + bfr(a0[1])), pk(lo16(hres[1]) + bfr(a0[2]), hi16(hres[1]) + bfr(a0[3]))};
      v2u o1 = {pk(lo16(hres[2]) + bfr(a1[0]), hi16(hres[2]) + bfr(a1[1])), pk(lo16(hres[3]) + bfr(a1[2]), hi16(hres[3]) + bfr(a1[3]))};
      v4u ov = join16(o0, o1);
      unsigned vo = (unsigned)(((g & 1) * 16 + c16) * 1024 + (g >> 1) * 16), so = (unsigned)((tj * 64 + team * 32) * 1024 + ch * 128 + hd * 32);
      GSTORE(ov, rout, vo, so);
    }
  }
}

extern "C" void kernel_launch(void* const* d_in, const int* in_sizes, int n_in, void* d_out, int out_size, void* d_ws, size_t ws_size,
                              hipStream_t stream) {
  (void)hipFuncSetAttribute((const void*)fused_block, hipFuncAttributeMaxDynamicSharedMemorySize, LDS_BYTES);
  fused_block<<<256, 512, LDS_BYTES, stream>>>(d_in[0], d_in[1], d_in[2], d_in[4], d_in[5], d_in[6], d_ws, d_out);
}
```
